```python
import torch

def _softmax(x, axis=-1):
    return torch.softmax(x, dim=axis)
def _gelu(x):
    return torch.nn.functional.gelu(x, approximate="tanh")
def _silu(x):
    return torch.nn.functional.silu(x)
def _sigmoid(x):
    return torch.sigmoid(x)
def _layernorm(x, axis=-1, eps=1e-5):
    m = x.mean(dim=axis, keepdim=True)
    v = x.var(dim=axis, keepdim=True, unbiased=False)
    return (x - m) / torch.sqrt(v + eps)
def _scatter_add(tgt, idx, src, axis=0):
    return tgt.clone().index_add_(axis, idx, src)
def _segment_sum(src, seg, n):
    out = torch.zeros((n,) + src.shape[1:], dtype=src.dtype)
    return out.index_add_(0, seg, src)
def _pad(x, pw):
    flat = tuple(v for pair in reversed(pw) for v in pair)
    return torch.nn.functional.pad(x, flat)

kernel_name = 'standalone__dense_activation_relu__prompt'

def setup_inputs():

    _w = (lambda a: (lambda t: t.to(torch.bfloat16) if t.is_floating_point() else t)(torch.as_tensor(a)))
    B, S, D, Dff = 1, 2048, 640, 1728
    return {
        "x": _w((lambda *s: torch.randn(*s, dtype=torch.bfloat16) * 0.05)(B, S, D)),
        "w1": _w((lambda *s: torch.randn(*s, dtype=torch.bfloat16) * 0.05)(D, Dff)),
        "w2": _w((lambda *s: torch.randn(*s, dtype=torch.bfloat16) * 0.05)(Dff, D)),
    }

def reference(x, w1, w2):
    h = torch.einsum("bsd,df->bsf", x, w1)
    zero = torch.zeros_like(h)
    h = torch.maximum(h, zero)
    return torch.einsum("bsf,fd->bsd", h, w2)
```

```cpp
#include <hip/hip_runtime.h>
#include <hip/hip_bf16.h>
typedef short s16x4 __attribute__((ext_vector_type(4)));
typedef short s16x8 __attribute__((ext_vector_type(8)));
typedef __bf16 bf16x8 __attribute__((ext_vector_type(8)));
typedef float f32x4 __attribute__((ext_vector_type(4)));
typedef unsigned u32x2 __attribute__((ext_vector_type(2)));
#define LDSP(T, p) ((__attribute__((address_space(3))) T*)(p))
#define GP(T, p) ((const __attribute__((address_space(1))) T*)(p))

__device__ __forceinline__ unsigned pk(float a, float b) {
  __hip_bfloat162 v = __float22bfloat162_rn(float2{a, b});
  return *reinterpret_cast<unsigned*>(&v);
}

template <int BM, int BN, bool RELU>
__global__ __launch_bounds__(256) void gemm(const unsigned short* __restrict__ A, const unsigned short* __restrict__ B,
                                            unsigned short* __restrict__ C, int M, int N, int K) {
  constexpr int ABYTES = BM * 128, BBYTES = BN * 128, STAGE = ABYTES + BBYTES;
  constexpr int WM = BM / 2, WN = BN / 2, MB = WM / 16, NB = WN / 16;
  __shared__ __attribute__((aligned(16))) char smem[2 * STAGE];
  const int tid = threadIdx.x, lane = tid & 63, wid = tid >> 6;
  const int wr = wid >> 1, wc = wid & 1;
  const int nbn = N / BN;
  const int bm = blockIdx.x / nbn, bn = blockIdx.x % nbn;
  const unsigned short* Ab = A + (size_t)bm * BM * K;
  const unsigned short* Bb = B + (size_t)bn * BN;
  auto stage = [&](int buf, int kt) {
    char* sa = smem + buf * STAGE;
    char* sb = sa + ABYTES;
#pragma unroll
    for (int i = 0; i < ABYTES / 4096; ++i) {
      int off = i * 4096 + tid * 16;
      int row = off >> 7, cp = (off >> 4) & 7, ch = cp ^ ((row >> 1) & 7);
      __builtin_amdgcn_global_load_lds(GP(unsigned, Ab + (size_t)row * K + kt * 64 + ch * 8), LDSP(unsigned, sa + i * 4096 + wid * 1024), 16, 0, 0);
    }
#pragma unroll
    for (int i = 0; i < BBYTES / 4096; ++i) {
      int off = i * 4096 + tid * 16;
      int panel = off >> 13, k = (off >> 7) & 63, cp = (off >> 4) & 7;
      int ch = cp ^ ((((k >> 1) & 1) << 1) | (((k >> 3) & 1) << 2));
      __builtin_amdgcn_global_load_lds(GP(unsigned, Bb + (size_t)(kt * 64 + k) * N + panel * 64 + ch * 8), LDSP(unsigned, sb + i * 4096 + wid * 1024), 16, 0, 0);
    }
  };
  f32x4 acc[MB][NB];
#pragma unroll
  for (int i = 0; i < MB; ++i)
#pragma unroll
    for (int j = 0; j < NB; ++j) acc[i][j] = f32x4{0.f, 0.f, 0.f, 0.f};
  const int nt = K / 64;
  stage(0, 0);
  asm volatile("s_waitcnt vmcnt(0)" ::: "memory");
  __syncthreads();
  const int g = lane >> 4, q = (lane & 15) >> 2, p = lane & 3, fr = lane & 15;
  for (int t = 0; t < nt; ++t) {
    int cur = t & 1;
    if (t + 1 < nt) stage(cur ^ 1, t + 1);
    char* sa = smem + cur * STAGE;
    char* sb = sa + ABYTES;
#pragma unroll
    for (int s = 0; s < 2; ++s) {
      bf16x8 af[MB], bf[NB];
#pragma unroll
      for (int i = 0; i < MB; ++i) {
        int row = wr * WM + i * 16 + fr, ch = 4 * s + g;
        af[i] = *reinterpret_cast<bf16x8*>(sa + row * 128 + ((ch ^ ((row >> 1) & 7)) << 4));
      }
#pragma unroll
      for (int j = 0; j < NB; ++j) {
        int nb = wc * NB + j;
        s16x4 r[2];
#pragma unroll
        for (int tt = 0; tt < 2; ++tt) {
          int k = 32 * s + 8 * g + 4 * tt + q;
          int ch = (nb & 3) * 2 + (p >> 1);
          int sw = (((k >> 1) & 1) << 1) | (((k >> 3) & 1) << 2);
          r[tt] = __builtin_amdgcn_ds_read_tr16_b64_v4i16(LDSP(s16x4, sb + (nb >> 2) * 8192 + k * 128 + ((ch ^ sw) << 4) + (p & 1) * 8));
        }
        s16x8 v = __builtin_shufflevector(r[0], r[1], 0, 1, 2, 3, 4, 5, 6, 7);
        bf[j] = __builtin_bit_cast(bf16x8, v);
      }
#pragma unroll
      for (int i = 0; i < MB; ++i)
#pragma unroll
        for (int j = 0; j < NB; ++j) acc[i][j] = __builtin_amdgcn_mfma_f32_16x16x32_bf16(bf[j], af[i], acc[i][j], 0, 0, 0);
    }
    asm volatile("s_waitcnt vmcnt(0)" ::: "memory");
    __syncthreads();
  }
#pragma unroll
  for (int i = 0; i < MB; ++i)
#pragma unroll
    for (int j = 0; j < NB; ++j) {
      f32x4 v = acc[i][j];
      if (RELU) { v[0] = fmaxf(v[0], 0.f); v[1] = fmaxf(v[1], 0.f); v[2] = fmaxf(v[2], 0.f); v[3] = fmaxf(v[3], 0.f); }
      u32x2 o = {pk(v[0], v[1]), pk(v[2], v[3])};
      int m = bm * BM + wr * WM + i * 16 + fr, n = bn * BN + wc * WN + j * 16 + 4 * g;
      *reinterpret_cast<u32x2*>(C + (size_t)m * N + n) = o;
    }
}

extern "C" void kernel_launch(void* const* d_in, const int* in_sizes, int n_in, void* d_out, int out_size, void* d_ws,
                              size_t ws_size, hipStream_t stream) {
  const unsigned short* x = (const unsigned short*)d_in[0];
  const unsigned short* w1 = (const unsigned short*)d_in[1];
  const unsigned short* w2 = (const unsigned short*)d_in[2];
  unsigned short* h = (unsigned short*)d_ws;
  unsigned short* out = (unsigned short*)d_out;
  const int M = 2048, D = 640, F = 1728;
  gemm<128, 64, true><<<(M / 128) * (F / 64), 256, 0, stream>>>(x, w1, h, M, F, D);
  gemm<64, 64, false><<<(M / 64) * (D / 64), 256, 0, stream>>>(h, w2, out, M, D, F);
}
```

```cpp
#include <hip/hip_runtime.h>
typedef short s16x4 __attribute__((ext_vector_type(4)));
typedef short s16x8 __attribute__((ext_vector_type(8)));
typedef __bf16 bf16x8 __attribute__((ext_vector_type(8)));
typedef __bf16 bf16x2 __attribute__((ext_vector_type(2)));
typedef float f32x2 __attribute__((ext_vector_type(2)));
typedef float f32x4 __attribute__((ext_vector_type(4)));
typedef float f32x16 __attribute__((ext_vector_type(16)));
typedef unsigned u32x2 __attribute__((ext_vector_type(2)));
typedef unsigned u32x4 __attribute__((ext_vector_type(4)));
typedef unsigned long long u64;
#define LAS __attribute__((address_space(3)))
#define GAS __attribute__((address_space(1)))
#define DI __device__ __forceinline__

constexpr int M_ = 2048, D_ = 640, F_ = 1728;
constexpr unsigned LDS_BYTES = 163840;
constexpr unsigned WS_EPOCH = 0;
constexpr unsigned WS_ACK = 4096;
constexpr unsigned WS_HFLAG = 8192;
constexpr unsigned WS_STAMP = 1u << 17;
constexpr unsigned WS_H = 1u << 20;
constexpr unsigned WS_END = WS_H + 64 * 108 * 1024;
constexpr unsigned SPIN_MAX = 1u << 18;

#define WAITV(n) asm volatile("s_waitcnt vmcnt(" #n ")" ::: "memory")
#define WAITL0() asm volatile("s_waitcnt lgkmcnt(0)" ::: "memory")
#define BAR() __builtin_amdgcn_s_barrier()
#define SB() __builtin_amdgcn_sched_barrier(0)
#define STAMP(k) do {} while (0)
#define LSTAMP(k) do {} while (0)

DI unsigned cvtpk(float lo, float hi) { f32x2 v = {lo, hi}; bf16x2 r = __builtin_convertvector(v, bf16x2); return __builtin_bit_cast(unsigned, r); }
DI bf16x8 tr2(unsigned a0, unsigned a1) {
  s16x4 lo = __builtin_amdgcn_ds_read_tr16_b64_v4i16((LAS s16x4*)a0);
  s16x4 hi = __builtin_amdgcn_ds_read_tr16_b64_v4i16((LAS s16x4*)a1);
  s16x8 v = __builtin_shufflevector(lo, hi, 0, 1, 2, 3, 4, 5, 6, 7);
  return __builtin_bit_cast(bf16x8, v);
}
DI bf16x8 lds128(unsigned a) { return *(LAS bf16x8*)a; }
#define MFMA(a, b, c) __builtin_amdgcn_mfma_f32_32x32x16_bf16((a), (b), (c), 0, 0, 0)
#define RLX_AGENT __ATOMIC_RELAXED, __HIP_MEMORY_SCOPE_AGENT
#define DMA(rsrc, ldsoff, voff, soff, aux) __builtin_amdgcn_raw_ptr_buffer_load_lds(rsrc, (LAS void*)(smem + (ldsoff)), 16, voff, soff, 0, aux)

__global__ __launch_bounds__(512, 2) void ffn_fused(const char* __restrict__ x, const char* __restrict__ w1, const char* __restrict__ w2,
                                                    char* __restrict__ out, char* __restrict__ ws) {
#if defined(__HIP_DEVICE_COMPILE__)
  extern __shared__ __attribute__((aligned(16))) char smem[];
  const int b = blockIdx.x, xr = (b & 7) >> 1, xc = b & 1, kk = b >> 3;
  const int lane = threadIdx.x & 63, wid = __builtin_amdgcn_readfirstlane(threadIdx.x >> 6);
  const bool has1 = kk < 28;
  const int i = 4 * xr + (kk & 3), cl = kk >> 2, ct = 7 * xc + cl;
  const int nrt = (xr & 1) ? 5 : 6, k2 = kk - ((xr & 1) ? 6 : 2);
  const bool has2 = k2 >= 0 && k2 < nrt * 5;
  const int rl = k2 / 5, nl = k2 - rl * 5, rt = (xr >> 1) * 11 + (xr & 1) * 6 + rl, nt = 5 * xc + nl;
  const int krot = (3 * (kk & 3) + cl * 3 / 2) % 10;
  const int urot = (5 * nl + 4 * rl + 9 * (xr & 1)) % 27;
  LSTAMP(0);
  __amdgpu_buffer_rsrc_t rws = __builtin_amdgcn_make_buffer_rsrc((void*)ws, 0, WS_END, 0x00020000);

  if (!has1 && !has2) {
    if (wid == 0) {
      const u64 tag = __hip_atomic_load((GAS u64*)(ws + WS_EPOCH), RLX_AGENT) + 1;
      if (lane == 0) __hip_atomic_store((GAS u64*)(ws + WS_ACK + b * 8), tag, RLX_AGENT);
      if (b == 255) {
        for (unsigned spin = 0; spin < SPIN_MAX; ++spin) {
          bool ok = true;
#pragma unroll
          for (int q = 0; q < 4; ++q) ok &= __hip_atomic_load((GAS u64*)(ws + WS_ACK + (q * 64 + lane) * 8), RLX_AGENT) == tag;
          if (__all(ok)) break;
          __builtin_amdgcn_s_sleep(8);
        }
        if (lane == 0) __hip_atomic_store((GAS u64*)(ws + WS_EPOCH), tag, RLX_AGENT);
      }
    }
    return;
  }

  if (wid >= 4) {
    const int L = wid - 4;
    u64 tag;
    asm volatile("global_load_dwordx2 %0, %1, %2 sc1" : "=v"(tag) : "v"(0), "s"(ws + WS_EPOCH) : "memory");
    __amdgpu_buffer_rsrc_t r2 = __builtin_amdgcn_make_buffer_rsrc((void*)w2, 0, F_ * D_ * 2, 0x00020000);
    const int v2 = (lane >> 3) * 1280 + nt * 128 + (((lane & 7) ^ (((lane >> 4) & 1) << 2)) << 4);
    auto wc = [&](int c, unsigned base) {
      int p = 4 * c + L, u = p + urot; if (u >= 27) u -= 27;
      const int vv = (p < 27) ? v2 : 0x7ffffff0;
      if (p >= 27) u = 0;
#pragma unroll
      for (int q = 0; q < 8; ++q) DMA(r2, base + L * 8192 + q * 1024, vv, (u * 64 + q * 8) * 1280, 0);
    };
    if (has1) {
      __amdgpu_buffer_rsrc_t rx = __builtin_amdgcn_make_buffer_rsrc((void*)x, 0, M_ * D_ * 2, 0x00020000);
      __amdgpu_buffer_rsrc_t r1 = __builtin_amdgcn_make_buffer_rsrc((void*)w1, 0, D_ * F_ * 2, 0x00020000);
      int vx[4];
#pragma unroll
      for (int q = 0; q < 4; ++q) { int row = 32 * L + 8 * q + (lane >> 3); vx[q] = (128 * i + row) * 1280 + (((lane & 7) ^ ((row >> 1) & 7)) << 4); }
      const int wch = (lane & 15) ^ (((lane >> 4) & 3) << 2);
      int v1 = (16 * L + (lane >> 4)) * 3456 + ct * 256 + wch * 16;
      if (ct == 13 && wch >= 8) v1 = 0x7ffffff0;
      int kb = krot; unsigned base = L * 4096u;
      auto g1 = [&]() {
#pragma unroll
        for (int q = 0; q < 4; ++q) DMA(rx, base + q * 1024, vx[q], kb * 128, 0);
#pragma unroll
        for (int q = 0; q < 4; ++q) DMA(r1, base + 16384 + q * 1024, v1, (kb * 64 + q * 4) * 3456, 0);
        kb = (kb == 9) ? 0 : kb + 1;
        base = (base >= 4 * 32768u) ? base - 4 * 32768u : base + 32768u;
      };
      g1(); g1();
#pragma unroll 1
      for (int v = 0; v < 8; ++v) { WAITV(8); BAR(); g1(); }
      WAITV(8); BAR();
      if (!has2) { WAITV(0); BAR(); return; }
      wc(0, 0);
      WAITV(8); BAR();
    } else {
      wc(0, 0);
    }
    LSTAMP(1);
    wc(1, 32768); wc(2, 65536);
    WAITV(8); BAR();
    wc(3, 98304); wc(4, 131072);
    WAITV(8); BAR();
    LSTAMP(2);
    wc(5, 0); wc(6, 32768);
    WAITV(16); BAR();
    asm volatile("" : "+v"(tag));
    tag += 1;
    const int mb0 = 3 * rt;
    {
      const int fb = lane >> 4, fc = lane & 15;
      const GAS u64* fp = (const GAS u64*)(ws + WS_HFLAG + min(mb0 + fb, 63) * 128 + fc * 8);
      for (unsigned spin = 0; spin < SPIN_MAX; ++spin) {
        u64 f = tag;
        if (fb < 3 && fc < 14) f = __hip_atomic_load(fp, RLX_AGENT);
        if (__all(f == tag)) break;
        __builtin_amdgcn_s_sleep(1);
      }
      __builtin_amdgcn_fence(__ATOMIC_ACQUIRE, "wavefront");
    }
    LSTAMP(3);
    unsigned hs0 = WS_H + (mb0 * 108 + L) * 1024, hs1 = WS_H + (min(mb0 + 1, 63) * 108 + L) * 1024, hs2 = WS_H + (min(mb0 + 2, 63) * 108 + L) * 1024;
    int hu = urot; unsigned hbase = 65536u + L * 1024u;
    auto hs = [&](bool second) {
#pragma unroll
      for (int e = 0; e < 2; ++e) {
        const unsigned o = (unsigned)hu * 4096u;
        const int vv = (e == 0 || second) ? lane * 16 : 0x7ffffff0;
        DMA(rws, hbase + e * 12288, vv, hs0 + o, 16);
        DMA(rws, hbase + e * 12288 + 4096, vv, hs1 + o, 16);
        DMA(rws, hbase + e * 12288 + 8192, vv, hs2 + o, 16);
        hu = (hu == 26) ? 0 : hu + 1;
      }
      hbase = (hbase >= 65536u + 3 * 24576u) ? hbase - 3 * 24576u : hbase + 24576u;
    };
    hs(true); hs(true);
    BAR();
#pragma unroll 1
    for (int g = 0; g < 12; ++g) { WAITV(6); BAR(); hs(g < 11); }
    WAITV(6); BAR();
    WAITV(0); BAR();
    LSTAMP(4);
    BAR();
    return;
  }
  const unsigned lds0 = (unsigned)(size_t)(LAS char*)smem;
  const int w = wid, r = lane & 31, hh = lane >> 5, blk = (lane >> 4) & 1, q4 = (lane & 15) >> 2, p4 = lane & 3;
  asm volatile("" ::"s"(out));
  const u64 tag = __hip_atomic_load((GAS u64*)(ws + WS_EPOCH), RLX_AGENT) + 1;
  asm volatile("" ::"v"(tag));
  if (has1) {
    f32x16 acc[4];
#pragma unroll
    for (int c = 0; c < 4; ++c)
#pragma unroll
      for (int e = 0; e < 16; ++e) acc[c][e] = 0.f;
    unsigned xa[4], wa[4];
#pragma unroll
    for (int ks = 0; ks < 4; ++ks) xa[ks] = lds0 + (32 * w + r) * 128 + (((2 * ks + hh) ^ ((r >> 1) & 7)) << 4);
#pragma unroll
    for (int c = 0; c < 4; ++c) wa[c] = lds0 + 16384 + (8 * hh + q4) * 256 + ((((c ^ q4) << 2) | (2 * blk + (p4 >> 1))) << 4) + (p4 & 1) * 8;
    bf16x8 X0, X1, W0[4], W1[4];
#define RW(W, c, base, ks) W[c] = tr2(wa[c] + (base) + (16 * (ks)) * 256, wa[c] + (base) + (16 * (ks) + 4) * 256)
#define KSTEP(Xc, Wc, Xn, Wn, base, ks, MID) do { \
      acc[0] = MFMA(Wc[0], Xc, acc[0]); SB(); MID; Xn = lds128(xa[ks] + (base)); RW(Wn, 0, base, ks); SB(); \
      acc[1] = MFMA(Wc[1], Xc, acc[1]); SB(); RW(Wn, 1, base, ks); SB(); \
      acc[2] = MFMA(Wc[2], Xc, acc[2]); SB(); RW(Wn, 2, base, ks); SB(); \
      acc[3] = MFMA(Wc[3], Xc, acc[3]); SB(); RW(Wn, 3, base, ks); SB(); } while (0)
    BAR();
    if (w == 0 && lane == 0) __hip_atomic_store((GAS u64*)(ws + WS_ACK + b * 8), tag, RLX_AGENT);
    LSTAMP(1);
    unsigned base = 0;
    SB(); X0 = lds128(xa[0]); SB(); RW(W0, 0, 0, 0); SB(); RW(W0, 1, 0, 0); SB(); RW(W0, 2, 0, 0); SB(); RW(W0, 3, 0, 0); SB();
#pragma unroll 1
    for (int v = 0; v < 9; ++v) {
      KSTEP(X0, W0, X1, W1, base, 1, );
      KSTEP(X1, W1, X0, W0, base, 2, );
      KSTEP(X0, W0, X1, W1, base, 3, );
      base = (base == 4 * 32768u) ? 0u : base + 32768u;
      KSTEP(X1, W1, X0, W0, base, 0, BAR());
    }
    KSTEP(X0, W0, X1, W1, base, 1, );
    KSTEP(X1, W1, X0, W0, base, 2, );
    KSTEP(X0, W0, X1, W1, base, 3, );
#pragma unroll
    for (int c = 0; c < 4; ++c) acc[c] = MFMA(W1[c], X1, acc[c]);
    LSTAMP(2);
    const unsigned dst = WS_H + ((i * 4 + w) * 108 + ct * 8) * 1024;
#pragma unroll
    for (int c = 0; c < 4; ++c) {
      if (c < 2 || ct < 13) {
#pragma unroll
        for (int h2 = 0; h2 < 2; ++h2) {
          u32x4 o;
#pragma unroll
          for (int e = 0; e < 4; ++e) o[e] = cvtpk(fmaxf(acc[c][8 * h2 + 2 * e], 0.f), fmaxf(acc[c][8 * h2 + 2 * e + 1], 0.f));
          __builtin_amdgcn_raw_buffer_store_b128(o, rws, lane * 16, dst + (c * 2 + h2) * 1024, 16);
        }
      }
    }
    if (!has2) {
      WAITV(0);
      if (lane == 0) __hip_atomic_store((GAS u64*)(ws + WS_HFLAG + (i * 4 + w) * 128 + ct * 8), tag, RLX_AGENT);
      return;
    }
  }
  const int kh = w >> 1, nb = w & 1;
  bf16x8 wr[40];
  const unsigned w2a = lds0 + (32 * kh + 4 * hh + q4) * 128 + ((((nb ^ (q4 >> 1)) << 2) | (2 * blk + (p4 >> 1))) << 4) + (p4 & 1) * 8;
#define RDC(c) do { _Pragma("unroll") for (int pp = 0; pp < 4; ++pp) _Pragma("unroll") for (int e = 0; e < 2; ++e) \
    wr[(4 * (c) + pp) * 2 + e] = tr2(w2a + (c) * 32768 + pp * 8192 + e * 2048, w2a + (c) * 32768 + pp * 8192 + e * 2048 + 1024); } while (0)
  BAR();
  if (!has1 && w == 0 && lane == 0) __hip_atomic_store((GAS u64*)(ws + WS_ACK + b * 8), tag, RLX_AGENT);
  RDC(0); RDC(1);
  WAITL0(); BAR();
  WAITV(0);
  if (has1 && lane == 0) __hip_atomic_store((GAS u64*)(ws + WS_HFLAG + (i * 4 + w) * 128 + ct * 8), tag, RLX_AGENT);
  LSTAMP(3);
  RDC(2); RDC(3);
  WAITL0(); BAR();
  RDC(4);
  WAITL0(); BAR();
  f32x16 a2[3];
#pragma unroll
  for (int c = 0; c < 3; ++c)
#pragma unroll
    for (int e = 0; e < 16; ++e) a2[c][e] = 0.f;
  const unsigned ha = lds0 + 65536 + kh * 2048 + lane * 16;
  bf16x8 H0[3], H1[3];
#define RH(H, m, hb, f) H[m] = lds128((hb) + ((f) >> 1) * 12288 + (m) * 4096 + ((f) & 1) * 1024)
#define HSTEP(Wf, Hc, Hn, hb, f, MID) do { \
    a2[0] = MFMA(Wf, Hc[0], a2[0]); SB(); MID; RH(Hn, 0, hb, f); SB(); \
    a2[1] = MFMA(Wf, Hc[1], a2[1]); SB(); RH(Hn, 1, hb, f); SB(); \
    a2[2] = MFMA(Wf, Hc[2], a2[2]); SB(); RH(Hn, 2, hb, f); SB(); } while (0)
  BAR();
  LSTAMP(4);
  RH(H0, 0, ha, 0); RH(H0, 1, ha, 0); RH(H0, 2, ha, 0);
  bf16x8 Wa, Wb;
#pragma unroll
  for (int g = 0; g < 10; ++g) {
    const unsigned hb = ha + (g & 3) * 24576, hn = ha + ((g + 1) & 3) * 24576;
    HSTEP(wr[4 * g], H0, H1, hb, 1, );
    HSTEP(wr[4 * g + 1], H1, H0, hb, 2, );
    HSTEP(wr[4 * g + 2], H0, H1, hb, 3, );
    if (g < 9) { HSTEP(wr[4 * g + 3], H1, H0, hn, 0, BAR()); }
    else { HSTEP(wr[4 * g + 3], H1, H0, hn, 0, BAR(); Wa = tr2(w2a, w2a + 1024)); }
  }
#define RWR(n) tr2(w2a + ((n) >> 1) * 8192 + ((n) & 1) * 2048, w2a + ((n) >> 1) * 8192 + ((n) & 1) * 2048 + 1024)
#pragma unroll
  for (int g = 10; g < 13; ++g) {
    const unsigned hb = ha + (g & 3) * 24576, hn = ha + ((g + 1) & 3) * 24576;
    const int n = 4 * (g - 10);
    HSTEP(Wa, H0, H1, hb, 1, Wb = RWR(n + 1));
    HSTEP(Wb, H1, H0, hb, 2, Wa = RWR(n + 2));
    HSTEP(Wa, H0, H1, hb, 3, Wb = RWR(n + 3));
    HSTEP(Wb, H1, H0, hn, 0, BAR(); Wa = RWR(n + 4));
  }
  {
    const unsigned hb = ha + (13 & 3) * 24576;
    HSTEP(Wa, H0, H1, hb, 1, Wb = RWR(13));
#pragma unroll
    for (int m = 0; m < 3; ++m) a2[m] = MFMA(Wb, H1[m], a2[m]);
  }
  LSTAMP(5);
  {
    const unsigned xb = lds0 + 65536 + 2 * 24576 + nb * 12288 + lane * 16;
#pragma unroll
    for (int t = 0; t < 3; ++t) {
#pragma unroll
      for (int e = 0; e < 2; ++e) {
        f32x4 lo, hi;
#pragma unroll
        for (int z = 0; z < 4; ++z) { lo[z] = a2[t >> 1][8 * (t & 1) + 4 * e + z]; hi[z] = a2[(3 + t) >> 1][8 * ((3 + t) & 1) + 4 * e + z]; }
        *(LAS f32x4*)(xb + kh * 6144 + (t * 2 + e) * 1024) = kh ? lo : hi;
      }
    }
    WAITL0(); BAR();
#pragma unroll
    for (int t = 0; t < 3; ++t) {
      unsigned g[4];
#pragma unroll
      for (int e = 0; e < 2; ++e) {
        f32x4 o = *(LAS f32x4*)(xb + (kh ^ 1) * 6144 + (t * 2 + e) * 1024);
        float v[4];
#pragma unroll
        for (int z = 0; z < 4; ++z) v[z] = o[z] + (kh ? a2[(3 + t) >> 1][8 * ((3 + t) & 1) + 4 * e + z] : a2[t >> 1][8 * (t & 1) + 4 * e + z]);
        g[2 * e] = cvtpk(v[0], v[1]); g[2 * e + 1] = cvtpk(v[2], v[3]);
      }
      auto s0 = __builtin_amdgcn_permlane32_swap(g[0], g[2], false, false);
      auto s1 = __builtin_amdgcn_permlane32_swap(g[1], g[3], false, false);
      u32x4 o = {s0[0], s1[0], s0[1], s1[1]};
      const int hbk = 3 * kh + t, mb = 3 * rt + (hbk >> 1);
      if (mb < 64) *(GAS u32x4*)(out + (size_t)(32 * mb + r) * 1280 + (64 * nt + 32 * nb + 16 * (hbk & 1) + 8 * hh) * 2) = o;
    }
  }
  LSTAMP(6);
#endif
}

extern "C" void kernel_launch(void* const* d_in, const int* in_sizes, int n_in, void* d_out, int out_size, void* d_ws,
                              size_t ws_size, hipStream_t stream) {
  (void)hipFuncSetAttribute((const void*)ffn_fused, hipFuncAttributeMaxDynamicSharedMemorySize, LDS_BYTES);
  ffn_fused<<<256, 512, LDS_BYTES, stream>>>((const char*)d_in[0], (const char*)d_in[1], (const char*)d_in[2], (char*)d_out, (char*)d_ws);
}
```

```cpp
#include <hip/hip_runtime.h>
#include <utility>
typedef short s16x4 __attribute__((ext_vector_type(4)));
typedef short s16x8 __attribute__((ext_vector_type(8)));
typedef __bf16 bf16x8 __attribute__((ext_vector_type(8)));
typedef __bf16 bf16x2 __attribute__((ext_vector_type(2)));
typedef float f32x2 __attribute__((ext_vector_type(2)));
typedef float f32x4 __attribute__((ext_vector_type(4)));
typedef float f32x16 __attribute__((ext_vector_type(16)));
typedef unsigned u32x2 __attribute__((ext_vector_type(2)));
typedef unsigned u32x4 __attribute__((ext_vector_type(4)));
typedef unsigned long long u64;
#define LAS __attribute__((address_space(3)))
#define GAS __attribute__((address_space(1)))
#define DI __device__ __forceinline__

constexpr int M_ = 2048, D_ = 640, F_ = 1728;
constexpr unsigned LDS_BYTES = 163840;
constexpr unsigned WS_EPOCH = 0;
constexpr unsigned WS_ACK = 4096;
constexpr unsigned WS_HFLAG = 8192;
constexpr unsigned WS_STAMP = 1u << 17;
constexpr unsigned WS_H = 1u << 20;
constexpr unsigned WS_END = WS_H + 64 * 108 * 1024;
constexpr unsigned SPIN_MAX = 1u << 16;
#define FLAGA_AT 58
#define SPRA 1
#define FLAGB_AT 12
#define PSLEEP 1
#define TSPLIT 135
constexpr int OOB = 0x7ffffff0;

#define WAITV(n) asm volatile("s_waitcnt vmcnt(" #n ")" ::: "memory")
#define WAITL0() asm volatile("s_waitcnt lgkmcnt(0)" ::: "memory")
#define BAR() __builtin_amdgcn_s_barrier()
#define SB() __builtin_amdgcn_sched_barrier(0)
#define LSTAMP(k) do {} while (0)
#define CSTAMP(k) do {} while (0)
#define DUMP() do {} while (0)

template <class F, int... Is> DI void sfor_(F&& f, std::integer_sequence<int, Is...>) { (f(std::integral_constant<int, Is>{}), ...); }
template <int N, class F> DI void sfor(F&& f) { sfor_(f, std::make_integer_sequence<int, N>{}); }
DI unsigned cvtpk(float lo, float hi) { f32x2 v = {lo, hi}; bf16x2 r = __builtin_convertvector(v, bf16x2); return __builtin_bit_cast(unsigned, r); }
DI bf16x8 tr2(unsigned a0, unsigned a1) {
  s16x4 lo = __builtin_amdgcn_ds_read_tr16_b64_v4i16((LAS s16x4*)a0);
  s16x4 hi = __builtin_amdgcn_ds_read_tr16_b64_v4i16((LAS s16x4*)a1);
  s16x8 v = __builtin_shufflevector(lo, hi, 0, 1, 2, 3, 4, 5, 6, 7);
  return __builtin_bit_cast(bf16x8, v);
}
DI bf16x8 lds128(unsigned a) { return *(LAS bf16x8*)a; }
#define MFMA(a, b, c) __builtin_amdgcn_mfma_f32_32x32x16_bf16((a), (b), (c), 0, 0, 0)
#define RLX_AGENT __ATOMIC_RELAXED, __HIP_MEMORY_SCOPE_AGENT
#define DMA(rsrc, ldsoff, voff, soff, aux) __builtin_amdgcn_raw_ptr_buffer_load_lds(rsrc, (LAS void*)(smem + (ldsoff)), 16, voff, soff, 0, aux)
__host__ __device__ constexpr int w2slot(int p) { return p < 7 ? p : p < 11 ? p + 9 : p < 17 ? p - 3 : p == 17 ? 7 : p < 20 ? p - 4 : p - 20; }
__host__ __device__ constexpr int w1bslot(int hv) { return hv < 8 ? hv : 6 + hv; }

__global__ __launch_bounds__(512, 2) void ffn_fused(const char* __restrict__ x, const char* __restrict__ w1, const char* __restrict__ w2,
                                                    char* __restrict__ out, char* __restrict__ ws) {
#if defined(__HIP_DEVICE_COMPILE__)
  extern __shared__ __attribute__((aligned(16))) char smem[];
  asm volatile("" ::"s"(x), "s"(w1), "s"(w2), "s"(out), "s"(ws));
  const int b = blockIdx.x, xr = (b & 7) >> 1, xc = b & 1, kk = b >> 3;
  const int lane = threadIdx.x & 63, wid = __builtin_amdgcn_readfirstlane(threadIdx.x >> 6);
  const bool has1 = kk < 28;
  const int i = 4 * xr + (kk & 3), cl = kk >> 2, ct = 7 * xc + cl;
  const int k2 = kk - 4 - (xr & 1), t2 = (xr >> 1) * 55 + (xr & 1) * 28 + k2;
  const bool has2 = k2 >= 0;
  const int rt = t2 / 5, nl = t2 - rt * 5, nt = 5 * xc + nl;
  LSTAMP(0);
  __amdgpu_buffer_rsrc_t rws = __builtin_amdgcn_make_buffer_rsrc((void*)ws, 0, WS_END, 0x00020000);

  if (wid >= 4) {
    const int L = wid - 4;
    u64 tag;
    asm volatile("global_load_dwordx2 %0, %1, %2 sc1" : "=v"(tag) : "v"(0), "s"(ws + WS_EPOCH) : "memory");
    const int wsw = ((lane & 7) ^ (((lane >> 4) & 1) << 2)) << 4;
    __amdgpu_buffer_rsrc_t r2 = __builtin_amdgcn_make_buffer_rsrc((void*)w2, 0, F_ * D_ * 2, 0x00020000);
    const int v2 = has2 ? ((lane >> 3) + 16 * L) * 1280 + nt * 128 + wsw : OOB;
    const int rotA = has2 ? (3 * nl + 2 * rt) % 14 : 0, rotB = has2 ? (nl + rt) % 6 : 0, rotC = has2 ? (nl + (nl >> 1) + rt) % 7 : 0;
    auto unit = [&](int p) {
      int a = p + rotA; if (a >= 14) a -= 14;
      int c = p - 14 + rotB; if (c >= 6) c -= 6;
      int d = p - 20 + rotC; if (d >= 7) d -= 7;
      return p < 14 ? 2 * a : p < 20 ? 2 * c + 1 : 2 * d + 13;
    };
    unsigned tslot = 7 * 8192;
    auto touch = [&](int t0, int t1, int step) {
#pragma unroll 1
      for (int t = t0; t < t1; t += step) {
        const int uo = t / 5, c = t - uo * 5, u = uo < 14 ? 2 * uo : 2 * (uo - 14) + 1;
        __builtin_amdgcn_raw_ptr_buffer_load_lds(r2, (LAS void*)(smem + tslot + L * 256), 4, lane * 1280, u * (64 * 1280) + xc * 640 + c * 128, 0, 0);
      }
    };
    auto wu = [&](int p) {
      const int so = unit(p) * (64 * 1280);
      DMA(r2, w2slot(p) * 8192 + L * 2048, v2, so, 0);
      DMA(r2, w2slot(p) * 8192 + L * 2048 + 1024, v2, so + 8 * 1280, 0);
    };
    if (has1) {
      __amdgpu_buffer_rsrc_t rx = __builtin_amdgcn_make_buffer_rsrc((void*)x, 0, M_ * D_ * 2, 0x00020000);
      __amdgpu_buffer_rsrc_t r1 = __builtin_amdgcn_make_buffer_rsrc((void*)w1, 0, D_ * F_ * 2, 0x00020000);
      int vx[4];
#pragma unroll
      for (int q = 0; q < 4; ++q) { int row = 32 * L + 8 * q + (lane >> 3); vx[q] = (128 * i + row) * 1280 + (((lane & 7) ^ ((row >> 1) & 7)) << 4); }
      const int v1 = ((lane >> 3) + 16 * L) * 3456 + ct * 256 + wsw;
      const int v1b = ct < 13 ? v1 + 128 : OOB;
      const int krot = (3 * (kk & 3) + cl * 3 / 2) % 10;
      int kb = krot; unsigned base = 65536u;
      auto g1 = [&]() {
#pragma unroll
        for (int q = 0; q < 4; ++q) DMA(rx, base + L * 4096 + q * 1024, vx[q], kb * 128, 0);
        DMA(r1, base + 16384 + L * 2048, v1, kb * (64 * 3456), 0);
        DMA(r1, base + 16384 + L * 2048 + 1024, v1, kb * (64 * 3456) + 8 * 3456, 0);
        kb = (kb == 9) ? 0 : kb + 1;
        base = (base == 65536u + 3 * 24576u) ? 65536u : base + 24576u;
      };
      int kc = krot;
      auto g1b = [&](int hv) {
        DMA(r1, w1bslot(hv) * 8192 + L * 2048, v1b, kc * (64 * 3456), 0);
        DMA(r1, w1bslot(hv) * 8192 + L * 2048 + 1024, v1b, kc * (64 * 3456) + 8 * 3456, 0);
        kc = (kc == 9) ? 0 : kc + 1;
      };
      g1(); g1(); g1b(0); g1b(1); g1b(2);
      WAITV(12); BAR(); g1();
      WAITV(12); BAR(); g1();
#pragma unroll 1
      for (int v = 2; v < 8; ++v) { WAITV(6); BAR(); g1(); }
      WAITV(6); BAR(); g1b(3); g1b(4); g1b(5); g1b(6);
      WAITV(8); BAR(); g1b(7); g1b(8); g1b(9); wu(7); wu(8); wu(9);
      LSTAMP(1);
      WAITV(12); BAR();
      if (!has2) {
        WAITV(0);
        tslot = 19 * 8192; if (kk < 4) touch(TSPLIT + kk * 4 + L, 135, 16);
        BAR(); BAR();
        WAITV(0);
        if (b == 0 && L == 0) {
          asm volatile("" : "+v"(tag));
          tag += 1;
          for (unsigned spin = 0; spin < SPIN_MAX; ++spin) {
            bool ok = true;
#pragma unroll
            for (int q = 0; q < 4; ++q) ok &= __hip_atomic_load((GAS u64*)(ws + WS_ACK + (q * 64 + lane) * 8), RLX_AGENT) == tag;
            if (__all(ok)) break;
            __builtin_amdgcn_s_sleep(8);
          }
          if (lane == 0) __hip_atomic_store((GAS u64*)(ws + WS_EPOCH), tag, RLX_AGENT);
        }
        DUMP();
        return;
      }
      wu(10); wu(11); wu(12); wu(13); wu(14); wu(15); wu(16);
      WAITV(20); BAR();
      wu(0); wu(1); wu(2); wu(3);
    } else {
      touch((kk - 28) * 4 + L, TSPLIT, 16);
      WAITV(4); BAR();
#pragma unroll
      for (int p = 7; p < 17; ++p) wu(p);
      wu(0); wu(1); wu(2); wu(3);
    }
    WAITV(8); BAR();
    LSTAMP(2);
    asm volatile("" : "+v"(tag));
    tag += 1;
    const int mb0 = 3 * rt;
    const bool fl0 = lane < 48 && (lane & 15) < 14, fl1 = lane < 48 && (lane & 15) < 13;
    const GAS u64* fp = (const GAS u64*)(ws + WS_HFLAG + min(mb0 + min(lane >> 4, 2), 63) * 128 + (lane & 15) * 8);
    u64 fs;
    auto poll = [&](int half) {
      if (!__all(fs == tag || !(half ? fl1 : fl0))) {
        for (unsigned spin = 0; spin < SPIN_MAX; ++spin) {
          const u64 f = __hip_atomic_load(fp + half * 1024, RLX_AGENT);
          if (__all(f == tag || !(half ? fl1 : fl0))) break;
          __builtin_amdgcn_s_sleep(PSLEEP);
        }
      }
      __builtin_amdgcn_fence(__ATOMIC_ACQUIRE, "wavefront");
    };
    wu(17); wu(18); wu(19);
    asm volatile("global_load_dwordx2 %0, %1, off sc1" : "=v"(fs) : "v"(fp) : "memory");
    wu(4); wu(5); wu(6);
    asm volatile("s_waitcnt vmcnt(6)" : "+v"(fs)::"memory");
    BAR();
    LSTAMP(7);
    poll(0);
    LSTAMP(3);
    const unsigned hs0 = WS_H + (mb0 * 108 + L) * 1024, hs1 = WS_H + (min(mb0 + 1, 63) * 108 + L) * 1024, hs2 = WS_H + (min(mb0 + 2, 63) * 108 + L) * 1024;
    unsigned hbase = 65536u + L * 1024u;
    auto hs = [&](int g) {
#pragma unroll
      for (int e = 0; e < 2; ++e) {
        const unsigned o = (unsigned)unit(min(2 * g + e, 26)) * 4096u;
        const int vv = (2 * g + e < 27) ? lane * 16 : OOB;
        DMA(rws, hbase + e * 12288, vv, hs0 + o, 16);
        DMA(rws, hbase + e * 12288 + 4096, vv, hs1 + o, 16);
        DMA(rws, hbase + e * 12288 + 8192, vv, hs2 + o, 16);
      }
      hbase = (hbase >= 65536u + 3 * 24576u) ? hbase - 3 * 24576u : hbase + 24576u;
    };
    hs(0); hs(1);
#pragma unroll 1
    for (int g = 0; g < 4; ++g) { WAITV(6); BAR(); hs(g + 2); }
    WAITV(6); BAR();
    asm volatile("global_load_dwordx2 %0, %1, off sc1" : "=v"(fs) : "v"(fp + 1024) : "memory");
    wu(20); wu(21); hs(6);
    asm volatile("s_waitcnt vmcnt(6)" : "+v"(fs)::"memory");
    BAR();
    LSTAMP(5); poll(1); LSTAMP(6);
    wu(22); wu(23); hs(7);
    WAITV(6); BAR(); wu(24); hs(8);
    WAITV(6); BAR(); wu(25); hs(9);
    WAITV(6); BAR(); wu(26); hs(10);
#pragma unroll 1
    for (int g = 9; g < 12; ++g) { WAITV(6); BAR(); hs(g + 2); }
    WAITV(6); BAR();
    WAITV(0); BAR();
    LSTAMP(4);
    DUMP();
    return;
  }
  const unsigned lds0 = (unsigned)(size_t)(LAS char*)smem;
  const int w = wid, r = lane & 31, hh = lane >> 5, blk = (lane >> 4) & 1, q4 = (lane & 15) >> 2, p4 = lane & 3;
  __builtin_amdgcn_s_setprio(3);
  const u64 tag = __hip_atomic_load((GAS u64*)(ws + WS_EPOCH), RLX_AGENT) + 1;
  asm volatile("" ::"v"(tag));
  const int fo = lane == 0 ? (int)(WS_HFLAG + (i * 4 + w) * 128 + ct * 8) : OOB;
#define FLAG(half) __builtin_amdgcn_raw_buffer_store_b64(__builtin_bit_cast(u32x2, tag), rws, fo, (half) * 8192, 16)
  const int fob = (has1 && ct < 13) ? fo : OOB;
  const int kh = w >> 1, nb = w & 1;
  bf16x8 wq[26];
  const unsigned w2a = lds0 + (32 * kh + 4 * hh + q4) * 128 + ((((nb ^ (q4 >> 1)) << 2) | (2 * (blk ^ kh) + (p4 >> 1))) << 4) + (p4 & 1) * 8;
#define RW2(n) tr2(w2a + w2slot((n) >> 1) * 8192 + ((n) & 1) * 2048, w2a + w2slot((n) >> 1) * 8192 + ((n) & 1) * 2048 + 1024)
  const unsigned hdst = WS_H + ((i * 4 + w) * 108 + ct * 8) * 1024;
  const int hvo = (has1 && ct < 13) ? lane * 16 : OOB;
  u32x4 hb[4] = {};
#define PUBB(f) __builtin_amdgcn_raw_buffer_store_b128(hb[f], rws, hvo, hdst + (4 + (f)) * 1024, 16)
  if (has1) {
    f32x16 aa[2], ab[2];
#pragma unroll
    for (int c = 0; c < 2; ++c)
#pragma unroll
      for (int e = 0; e < 16; ++e) { aa[c][e] = 0.f; ab[c][e] = 0.f; }
    unsigned xa[4], wa[2];
#pragma unroll
    for (int ks = 0; ks < 4; ++ks) xa[ks] = lds0 + 65536 + (32 * w + r) * 128 + (((2 * ks + hh) ^ ((r >> 1) & 7)) << 4);
#pragma unroll
    for (int c = 0; c < 2; ++c) wa[c] = lds0 + (8 * hh + q4) * 128 + ((((c ^ (q4 >> 1)) << 2) | (2 * blk + (p4 >> 1))) << 4) + (p4 & 1) * 8;
    bf16x8 xf[40], W[2][2];
#define RW(s, c, off) W[s][c] = tr2(wa[c] + (off), wa[c] + (off) + 512)
#define PUB(acc, f, half) do { u32x4 o_; _Pragma("unroll") for (int e = 0; e < 4; ++e) \
      o_[e] = cvtpk(fmaxf(acc[(f) >> 1][8 * ((f) & 1) + 2 * e], 0.f), fmaxf(acc[(f) >> 1][8 * ((f) & 1) + 2 * e + 1], 0.f)); \
      __builtin_amdgcn_raw_buffer_store_b128(o_, rws, lane * 16, hdst + ((half) * 4 + (f)) * 1024, 16); } while (0)
    BAR();
    if (w == 0 && lane == 0) __hip_atomic_store((GAS u64*)(ws + WS_ACK + b * 8), tag, RLX_AGENT);
    LSTAMP(1); CSTAMP(12);
#define WOFF(m) ((m) < 40 ? 65536 + (((m) >> 2) & 3) * 24576 + 16384 + ((m) & 3) * 2048 : w1bslot(((m) - 40) >> 2) * 8192 + ((m) & 3) * 2048)
    SB(); xf[0] = lds128(xa[0]); SB(); RW(0, 0, WOFF(0)); SB(); RW(0, 1, WOFF(0)); SB();
    xf[1] = lds128(xa[1]); SB(); RW(1, 0, WOFF(1)); SB(); RW(1, 1, WOFF(1)); SB();
    sfor<80>([&](auto nc) {
      constexpr int n = decltype(nc)::value, m = n + 2, s = n & 1;
      if constexpr (n < 40) aa[0] = MFMA(W[s][0], xf[n], aa[0]); else ab[0] = MFMA(W[s][0], xf[n - 40], ab[0]);
      SB();
      if constexpr (m < 40 ? (m & 3) == 0 : (m == 52 || m == 68)) BAR();
      if constexpr (m < 40) xf[m] = lds128(xa[m & 3] + ((m >> 2) & 3) * 24576);
      if constexpr (m < 80) RW(s, 0, WOFF(m));
      SB();
      if constexpr (n < 40) aa[1] = MFMA(W[s][1], xf[n], aa[1]); else ab[1] = MFMA(W[s][1], xf[n - 40], ab[1]);
      SB();
      if constexpr (m < 80) RW(s, 1, WOFF(m));
      if constexpr (n >= 41 && n < 41 + 4 * SPRA && (n - 41) % SPRA == 0) PUB(aa, (n - 41) / SPRA, 0);
      if constexpr (n == 40) { LSTAMP(7); }
      if constexpr (n == FLAGA_AT) { WAITV(0); FLAG(0); LSTAMP(8); }
      if constexpr (n == 78) { WAITL0(); BAR(); }
      SB();
    });
    LSTAMP(2); CSTAMP(13);
#pragma unroll
    for (int f = 0; f < 4; ++f)
#pragma unroll
      for (int e = 0; e < 4; ++e) hb[f][e] = cvtpk(fmaxf(ab[f >> 1][8 * (f & 1) + 2 * e], 0.f), fmaxf(ab[f >> 1][8 * (f & 1) + 2 * e + 1], 0.f));
    if (!has2) {
      PUBB(0); PUBB(1); PUBB(2); PUBB(3);
      WAITV(0);
      if (ct < 13) FLAG(1);
      DUMP();
      return;
    }
  } else {
    BAR();
    if (w == 0 && lane == 0) __hip_atomic_store((GAS u64*)(ws + WS_ACK + b * 8), tag, RLX_AGENT);
    BAR();
  }
  PUBB(0); PUBB(1);
#pragma unroll
  for (int n = 0; n < 10; ++n) wq[n] = RW2(14 + n);
  SB(); PUBB(2); PUBB(3); SB();
#pragma unroll
  for (int n = 10; n < 20; ++n) wq[n] = RW2(14 + n);
  WAITL0(); BAR();
#pragma unroll
  for (int n = 20; n < 26; ++n) wq[n] = RW2(14 + n);
  WAITV(0);
  __builtin_amdgcn_raw_buffer_store_b64(__builtin_bit_cast(u32x2, tag), rws, fob, 8192, 16);
  LSTAMP(3);
  WAITL0();
  f32x16 a2[3];
#pragma unroll
  for (int c = 0; c < 3; ++c)
#pragma unroll
    for (int e = 0; e < 16; ++e) a2[c][e] = 0.f;
  const unsigned ha1 = lds0 + 65536 + kh * 2048 + lane * 16 + 4096, ha0 = ha1 + (kh ? 4096 : -4096), ha2 = ha1 + (kh ? -4096 : 4096);
  bf16x8 H[3][3], Wr[3];
#define RH(m, n) H[(n) % 3][m] = lds128(((m) == 0 ? ha0 : (m) == 1 ? ha1 : ha2) + (((n) >> 2) & 3) * 24576 + (((n) >> 1) & 1) * 12288 + ((n) & 1) * 1024)
  BAR();
  LSTAMP(4); CSTAMP(14);
  SB(); Wr[0] = RW2(0); SB(); RH(0, 0); SB(); RH(1, 0); SB(); RH(2, 0); SB(); Wr[1] = RW2(1); SB(); RH(0, 1); SB(); RH(1, 1); SB(); RH(2, 1); SB();
  sfor<54>([&](auto nc) {
    constexpr int n = decltype(nc)::value, m = n + 2, s = n % 3;
    const bf16x8 wf = (n >= 14 && n < 40) ? wq[(n >= 14 && n < 40) ? n - 14 : 0] : Wr[s];
    a2[0] = MFMA(wf, H[s][0], a2[0]); SB();
    if constexpr (m < 54 && (m & 3) == 0) BAR();
    if constexpr (m < 14 || (m >= 40 && m < 54)) Wr[m % 3] = RW2(m);
    if constexpr (m < 54) RH(0, m);
    SB();
    a2[1] = MFMA(wf, H[s][1], a2[1]); SB();
    if constexpr (m < 54) RH(1, m);
    SB();
    a2[2] = MFMA(wf, H[s][2], a2[2]); SB();
    if constexpr (m < 54) RH(2, m);
    SB();
  });
  LSTAMP(5); CSTAMP(15);
  {
    const unsigned xb = lds0 + 65536 + 2 * 24576 + nb * 12288 + lane * 16;
#pragma unroll
    for (int t = 0; t < 6; ++t) {
      f32x4 v;
#pragma unroll
      for (int z = 0; z < 4; ++z) v[z] = t < 4 ? a2[2][4 * t + z] : a2[1][8 + 4 * (t - 4) + z];
      *(LAS f32x4*)(xb + kh * 6144 + t * 1024) = v;
    }
    __amdgpu_buffer_rsrc_t ro = __builtin_amdgcn_make_buffer_rsrc((void*)out, 0, M_ * D_ * 2, 0x00020000);
    const int ob = (96 * rt + r) * 1280 + (64 * nt + 32 * nb + 8 * hh) * 2;
    WAITL0(); BAR();
    f32x4 o[6];
#pragma unroll
    for (int t = 0; t < 6; ++t) o[t] = *(LAS f32x4*)(xb + (kh ^ 1) * 6144 + (t < 4 ? (t ^ 2) : t) * 1024);
#pragma unroll
    for (int t = 0; t < 3; ++t) {
      unsigned g[4];
#pragma unroll
      for (int e = 0; e < 2; ++e) {
        float v[4];
#pragma unroll
        for (int z = 0; z < 4; ++z) v[z] = o[2 * t + e][z] + (t < 2 ? a2[0][8 * t + 4 * e + z] : a2[1][4 * e + z]);
        g[2 * e] = cvtpk(v[0], v[1]); g[2 * e + 1] = cvtpk(v[2], v[3]);
      }
      auto s0 = __builtin_amdgcn_permlane32_swap(g[0], g[2], false, false);
      auto s1 = __builtin_amdgcn_permlane32_swap(g[1], g[3], false, false);
      u32x4 ov = {s0[0], s1[0], s0[1], s1[1]};
      const int mrow = t < 2 ? 2 * kh : 1, chalf = (t & 1) ^ kh;
      __builtin_amdgcn_raw_buffer_store_b128(ov, ro, ob + mrow * (32 * 1280) + chalf * 32, 0, 0);
    }
  }
  LSTAMP(6);
  DUMP();
#endif
}

extern "C" void kernel_launch(void* const* d_in, const int* in_sizes, int n_in, void* d_out, int out_size, void* d_ws,
                              size_t ws_size, hipStream_t stream) {
  (void)hipFuncSetAttribute((const void*)ffn_fused, hipFuncAttributeMaxDynamicSharedMemorySize, LDS_BYTES);
  ffn_fused<<<256, 512, LDS_BYTES, stream>>>((const char*)d_in[0], (const char*)d_in[1], (const char*)d_in[2], (char*)d_out, (char*)d_ws);
}
```

```cpp
#include <hip/hip_runtime.h>
#include <utility>
typedef short s16x4 __attribute__((ext_vector_type(4)));
typedef short s16x8 __attribute__((ext_vector_type(8)));
typedef __bf16 bf16x8 __attribute__((ext_vector_type(8)));
typedef __bf16 bf16x2 __attribute__((ext_vector_type(2)));
typedef float f32x2 __attribute__((ext_vector_type(2)));
typedef float f32x4 __attribute__((ext_vector_type(4)));
typedef float f32x16 __attribute__((ext_vector_type(16)));
typedef unsigned u32x2 __attribute__((ext_vector_type(2)));
typedef unsigned u32x4 __attribute__((ext_vector_type(4)));
typedef unsigned long long u64;
#define LAS __attribute__((address_space(3)))
#define GAS __attribute__((address_space(1)))
#define DI __device__ __forceinline__

constexpr int M_ = 2048, D_ = 640, F_ = 1728;
constexpr unsigned LDS_BYTES = 163840;
constexpr unsigned WS_EPOCH = 0;
constexpr unsigned WS_ACK = 4096;
constexpr unsigned WS_HFLAG = 8192;
constexpr unsigned WS_STAMP = 1u << 17;
constexpr unsigned WS_H = 1u << 20;
constexpr unsigned WS_END = WS_H + 64 * 108 * 1024;
constexpr unsigned SPIN_MAX = 1u << 16;
#define FLAGA_AT 144
#define SPRA 1
#define FLAGB_AT 12
#define PSLEEP 1
#define TSPLIT 135
constexpr int CODE_BYTES = 21504;
#define OAUX 16
constexpr int OOB = 0x7ffffff0;

#define WAITV(n) asm volatile("s_waitcnt vmcnt(" #n ")" ::: "memory")
#define WAITL0() asm volatile("s_waitcnt lgkmcnt(0)" ::: "memory")
#define BAR() __builtin_amdgcn_s_barrier()
#define SB() __builtin_amdgcn_sched_barrier(0)
#define LSTAMP(k) do {} while (0)
#define CSTAMP(k) do {} while (0)
#define DUMP() do {} while (0)

template <class F, int... Is> DI void sfor_(F&& f, std::integer_sequence<int, Is...>) { (f(std::integral_constant<int, Is>{}), ...); }
template <int N, class F> DI void sfor(F&& f) { sfor_(f, std::make_integer_sequence<int, N>{}); }
DI unsigned cvtpk(float lo, float hi) { f32x2 v = {lo, hi}; bf16x2 r = __builtin_convertvector(v, bf16x2); return __builtin_bit_cast(unsigned, r); }
DI bf16x8 tr2(unsigned a0, unsigned a1) {
  s16x4 lo = __builtin_amdgcn_ds_read_tr16_b64_v4i16((LAS s16x4*)a0);
  s16x4 hi = __builtin_amdgcn_ds_read_tr16_b64_v4i16((LAS s16x4*)a1);
  s16x8 v = __builtin_shufflevector(lo, hi, 0, 1, 2, 3, 4, 5, 6, 7);
  return __builtin_bit_cast(bf16x8, v);
}
DI bf16x8 lds128(unsigned a) { return *(LAS bf16x8*)a; }
#define MFMA(a, b, c) __builtin_amdgcn_mfma_f32_16x16x32_bf16((a), (b), (c), 0, 0, 0)
#define RLX_AGENT __ATOMIC_RELAXED, __HIP_MEMORY_SCOPE_AGENT
#define DMA(rsrc, ldsoff, voff, soff, aux) __builtin_amdgcn_raw_ptr_buffer_load_lds(rsrc, (LAS void*)(smem + (ldsoff)), 16, voff, soff, 0, aux)
__host__ __device__ constexpr int unit(int p) { return p < 14 ? 2 * p : 2 * (p - 14) + 1; }
#define NBV 3
constexpr int NB = NBV, NOP1 = 80 + 8 * NB;
struct Op { int v, ks, half, c, first; };
__host__ __device__ constexpr Op op1(int j) {
  if (j >= NOP1) { const int r = j - NOP1; return Op{NB + (r >> 3), (r >> 1) & 3, 1, r & 1, 0}; }
  if (j < 16) return Op{j >> 3, (j >> 1) & 3, 0, j & 1, !(j & 1)};
  if (j < 16 + 16 * NB) { const int r = j - 16, st = 2 + (r >> 4), h = (r >> 1) & 1; return Op{h ? st - 2 : st, (r >> 2) & 3, h, r & 1, (r & 3) == 0}; }
  const int r = j - 16 - 16 * NB; return Op{2 + NB + (r >> 3), (r >> 1) & 3, 0, r & 1, !(r & 1)};
}
__host__ __device__ constexpr int w2slot(int p) { return p < 7 ? p : p < 11 ? p + 9 : p < 17 ? p - 3 : p == 17 ? 7 : p < 20 ? p - 4 : p - 20; }
__host__ __device__ constexpr int w1bslot(int hv) { return hv < 8 ? hv : 6 + hv; }

DI void helper(int b, int lane, int wid, const char* w2, char* ws, char* smem) {
#if defined(__HIP_DEVICE_COMPILE__)
  const int xr = (b & 7) >> 1, xc = b & 1, hw = wid * 4 + ((b >> 3) - 28);
  LAS void* sink = (LAS void*)(smem + wid * 256);
  if (hw < 3) {
    u64 pc; unsigned po;
    asm volatile("s_getpc_b64 %0\n.Lpc%=:\n\ts_mov_b32 %1, .Lpc%=-ffn_fused" : "=s"(pc), "=s"(po));
    __amdgpu_buffer_rsrc_t rc = __builtin_amdgcn_make_buffer_rsrc((void*)(pc - po), 0, CODE_BYTES, 0x00020000);
    __builtin_amdgcn_raw_ptr_buffer_load_lds(rc, sink, 4, (hw * 64 + lane) * 128, 0, 0, 0);
  }
  const u64 tag = __hip_atomic_load((GAS u64*)(ws + WS_EPOCH), RLX_AGENT) + 1;
  asm volatile("" ::"v"(tag));
  BAR();
  if (wid == 0 && lane == 0) __hip_atomic_store((GAS u64*)(ws + WS_ACK + b * 8), tag, RLX_AGENT);
  __amdgpu_buffer_rsrc_t r2 = __builtin_amdgcn_make_buffer_rsrc((void*)w2, 0, F_ * D_ * 2, 0x00020000);
#pragma unroll 1
  for (int t = hw; t < 135; t += 32) {
    const int uo = t / 5, c = t - uo * 5, u = unit(uo < 10 ? uo + 7 : uo < 17 ? uo - 10 : uo);
    __builtin_amdgcn_raw_ptr_buffer_load_lds(r2, sink, 4, lane * 1280, u * (64 * 1280) + xc * 640 + c * 128, 0, 0);
  }
  if (b == 255 && wid == 7) {
    for (unsigned spin = 0; spin < SPIN_MAX; ++spin) {
      bool ok = true;
#pragma unroll
      for (int q = 0; q < 4; ++q) ok &= __hip_atomic_load((GAS u64*)(ws + WS_ACK + (q * 64 + lane) * 8), RLX_AGENT) == tag;
      if (__all(ok)) break;
      __builtin_amdgcn_s_sleep(8);
    }
    if (lane == 0) __hip_atomic_store((GAS u64*)(ws + WS_EPOCH), tag, RLX_AGENT);
  }
  WAITV(0);
#endif
}

extern "C" __global__ __launch_bounds__(512, 2) void ffn_fused(const char* __restrict__ x, const char* __restrict__ w1, const char* __restrict__ w2,
                                                    char* __restrict__ out, char* __restrict__ ws) {
#if defined(__HIP_DEVICE_COMPILE__)
  extern __shared__ __attribute__((aligned(16))) char smem[];
  const int b = blockIdx.x, xr = (b & 7) >> 1, xc = b & 1, kk = b >> 3;
  const int lane = threadIdx.x & 63, wid = __builtin_amdgcn_readfirstlane(threadIdx.x >> 6);
  if (__builtin_expect(kk >= 28, 0)) { helper(b, lane, wid, w2, ws, smem); return; }
  const int i = 4 * xr + (kk & 3), cl = kk >> 2, ct = 7 * xc + cl;
  const int L = wid & 3;
  const int wsw = ((lane & 7) ^ (((lane >> 4) & 1) << 1)) << 4;
  int krot = 3 * (kk & 3) + ((cl * 3) >> 1); krot -= krot >= 10 ? 10 : 0;
  const int vx0 = (128 * i + 32 * L + (lane >> 3)) * 1280 + (((lane & 7) ^ (lane >> 4)) << 4), vx1 = (vx0 + 8 * 1280) ^ 64;
  const int v1 = ((lane >> 3) + 16 * L) * 3456 + ct * 256 + wsw;
  __amdgpu_buffer_rsrc_t rx = __builtin_amdgcn_make_buffer_rsrc((void*)x, 0, M_ * D_ * 2, 0x00020000);
  __amdgpu_buffer_rsrc_t r1 = __builtin_amdgcn_make_buffer_rsrc((void*)w1, 0, D_ * F_ * 2, 0x00020000);
  int kb = krot + (wid >> 2); kb -= kb >= 10 ? 10 : 0;
  unsigned base = 65536u + (wid >> 2) * 24576u;
  auto g1 = [&]() {
    DMA(rx, base + L * 4096, vx0, kb * 128, 0);
    DMA(rx, base + L * 4096 + 1024, vx1, kb * 128, 0);
    DMA(rx, base + L * 4096 + 2048, vx0, kb * 128 + 16 * 1280, 0);
    DMA(rx, base + L * 4096 + 3072, vx1, kb * 128 + 16 * 1280, 0);
    DMA(r1, base + 16384 + L * 2048, v1, kb * (64 * 3456), 0);
    DMA(r1, base + 16384 + L * 2048 + 1024, v1 ^ 64, kb * (64 * 3456) + 8 * 3456, 0);
    kb = (kb == 9) ? 0 : kb + 1;
    base = (base == 65536u + 3 * 24576u) ? 65536u : base + 24576u;
  };
  g1();
  SB();
  asm volatile("" ::"s"(w2), "s"(out), "s"(ws));
  const int t2 = (xr >> 1) * 55 + (xr & 1) * 28 + kk;
  const bool has2 = kk < 28 - (xr & 1);
  const int rt = t2 / 5, nl = t2 - rt * 5, nt = 5 * xc + nl;
  LSTAMP(0);
  __amdgpu_buffer_rsrc_t rws = __builtin_amdgcn_make_buffer_rsrc((void*)ws, 0, WS_END, 0x00020000);

  if (__builtin_expect(wid >= 4, 1)) {
    u64 tag;
    asm volatile("global_load_dwordx2 %0, %1, %2 sc1" : "=v"(tag) : "v"(0), "s"(ws + WS_EPOCH) : "memory");
    __amdgpu_buffer_rsrc_t r2 = __builtin_amdgcn_make_buffer_rsrc((void*)w2, 0, F_ * D_ * 2, 0x00020000);
    const int v2 = has2 ? ((lane >> 3) + 16 * L) * 1280 + nt * 128 + (((lane & 7) ^ ((lane >> 4) << 1)) << 4) : OOB;
    auto wu = [&](int p) {
      const int so = unit(p) * (64 * 1280);
      DMA(r2, w2slot(p) * 8192 + L * 2048, v2, so, 0);
      DMA(r2, w2slot(p) * 8192 + L * 2048 + 1024, v2, so + 8 * 1280, 0);
    };
    const int v1b = ct < 13 ? v1 + 128 : OOB;
    int kc = krot;
    auto g1b = [&](int hv) {
      DMA(r1, w1bslot(hv) * 8192 + L * 2048, v1b, kc * (64 * 3456), 0);
      DMA(r1, w1bslot(hv) * 8192 + L * 2048 + 1024, v1b ^ 64, kc * (64 * 3456) + 8 * 3456, 0);
      kc = (kc == 9) ? 0 : kc + 1;
    };
    g1b(0); g1b(1); g1b(2);
    BAR(); g1();
    WAITV(12); BAR(); g1();
#pragma unroll 1
    for (int v = 2; v < 8; ++v) { WAITV(6); BAR(); g1(); }
    WAITV(6); BAR(); g1b(3); g1b(4); g1b(5); g1b(6);
    WAITV(8); BAR(); g1b(7); g1b(8);
    LSTAMP(1);
    WAITV(8); BAR(); g1b(9); wu(7); wu(8); wu(9);
    WAITV(6); BAR();
    if (!has2) { WAITV(0); DUMP(); return; }
    wu(10); wu(11); wu(12); wu(13); wu(14); wu(15); wu(16); wu(0); wu(1); wu(2);
    WAITV(6); BAR();
    LSTAMP(2);
    asm volatile("" : "+v"(tag));
    tag += 1;
    const int mb0 = 3 * rt;
    const bool fl0 = lane < 48 && (lane & 15) < 14, fl1 = lane < 48 && (lane & 15) < 13;
    const GAS u64* fp = (const GAS u64*)(ws + WS_HFLAG + min(mb0 + min(lane >> 4, 2), 63) * 128 + (lane & 15) * 8);
    u64 fs;
    auto poll = [&](int half) {
      if (!__all(fs == tag || !(half ? fl1 : fl0))) {
        for (unsigned spin = 0; spin < SPIN_MAX; ++spin) {
          const u64 f = __hip_atomic_load(fp + half * 1024, RLX_AGENT);
          if (__all(f == tag || !(half ? fl1 : fl0))) break;
          __builtin_amdgcn_s_sleep(PSLEEP);
        }
      }
      __builtin_amdgcn_fence(__ATOMIC_ACQUIRE, "wavefront");
    };
    wu(17); wu(18); wu(19);
    wu(3); wu(4);
    asm volatile("global_load_dwordx2 %0, %1, off sc1" : "=v"(fs) : "v"(fp) : "memory");
    wu(5); wu(6);
    WAITV(5);
    BAR();
    asm volatile("s_waitcnt vmcnt(4)" : "+v"(fs)::"memory");
    LSTAMP(7);
    poll(0);
    LSTAMP(3);
    const unsigned hs0 = WS_H + (mb0 * 108 + L) * 1024, hs1 = WS_H + (min(mb0 + 1, 63) * 108 + L) * 1024, hs2 = WS_H + (min(mb0 + 2, 63) * 108 + L) * 1024;
    unsigned hbase = 65536u + L * 1024u;
    auto hs = [&](int g) {
#pragma unroll
      for (int e = 0; e < 2; ++e) {
        const unsigned o = (unsigned)unit(min(2 * g + e, 26)) * 4096u;
        const int vv = (2 * g + e < 27) ? lane * 16 : OOB;
        DMA(rws, hbase + e * 12288, vv, hs0 + o, 16);
        DMA(rws, hbase + e * 12288 + 4096, vv, hs1 + o, 16);
        DMA(rws, hbase + e * 12288 + 8192, vv, hs2 + o, 16);
      }
      hbase = (hbase >= 65536u + 3 * 24576u) ? hbase - 3 * 24576u : hbase + 24576u;
    };
    hs(0); hs(1);
#pragma unroll 1
    for (int g = 0; g < 4; ++g) { WAITV(6); BAR(); hs(g + 2); }
    WAITV(6); BAR();
    asm volatile("global_load_dwordx2 %0, %1, off sc1" : "=v"(fs) : "v"(fp + 1024) : "memory");
    wu(20); wu(21); hs(6);
    asm volatile("s_waitcnt vmcnt(6)" : "+v"(fs)::"memory");
    BAR();
    LSTAMP(5); poll(1); LSTAMP(6);
    wu(22); wu(23); hs(7);
    WAITV(6); BAR(); wu(24); hs(8);
    WAITV(6); BAR(); wu(25); hs(9);
    WAITV(6); BAR(); wu(26); hs(10);
#pragma unroll 1
    for (int g = 9; g < 12; ++g) { WAITV(6); BAR(); hs(g + 2); }
    WAITV(6); BAR();
    WAITV(0); BAR();
    LSTAMP(4);
    DUMP();
    return;
  }
  const unsigned lds0 = (unsigned)(size_t)(LAS char*)smem;
  const int w = wid, fr = lane & 15, g4 = lane >> 4, q4 = fr >> 2, p4 = lane & 3, swb = (q4 >> 1) | ((g4 & 1) << 1);
  __builtin_amdgcn_s_setprio(3);
  const u64 tag = __hip_atomic_load((GAS u64*)(ws + WS_EPOCH), RLX_AGENT) + 1;
  asm volatile("" ::"v"(tag));
  const int fo = lane == 0 ? (int)(WS_HFLAG + (i * 4 + w) * 128 + ct * 8) : OOB;
#define FLAG(half) __builtin_amdgcn_raw_buffer_store_b64(__builtin_bit_cast(u32x2, tag), rws, fo, (half) * 8192, 16)
  const int fob = (ct < 13) ? fo : OOB;
  const int kh = w >> 1, nb = w & 1;
  bf16x8 wq[26];
  const unsigned w2a = lds0 + (32 * kh + 4 * g4 + q4) * 128 + (((((2 * nb) ^ swb) << 1) | (p4 >> 1)) << 4) + (p4 & 1) * 8, w2b = w2a ^ 32;
#define RW2(n) tr2((((n) & 1) ? w2b : w2a) + w2slot((n) >> 1) * 8192, (((n) & 1) ? w2b : w2a) + w2slot((n) >> 1) * 8192 + 2048)
  const unsigned hdst = WS_H + ((i * 4 + w) * 108 + ct * 8) * 1024;
  const int hvo = (ct < 13) ? lane * 16 : OOB;
  u32x4 hb[4] = {};
#define PUBB(f) __builtin_amdgcn_raw_buffer_store_b128(hb[f], rws, hvo, hdst + (4 + (f)) * 1024, 16)
  {
    f32x4 aa[4][2], ab[4][2];
#pragma unroll
    for (int c = 0; c < 8; ++c) { aa[c >> 1][c & 1] = f32x4{0.f, 0.f, 0.f, 0.f}; ab[c >> 1][c & 1] = f32x4{0.f, 0.f, 0.f, 0.f}; }
    unsigned xa[2], wa[4];
#pragma unroll
    for (int ks = 0; ks < 2; ++ks) xa[ks] = lds0 + 65536 + (32 * w + fr) * 128 + (((4 * ks + g4) ^ (fr >> 1)) << 4);
#pragma unroll
    for (int c = 0; c < 4; ++c) wa[c] = lds0 + (8 * g4 + q4) * 128 + ((((c ^ swb) << 1) | (p4 >> 1)) << 4) + (p4 & 1) * 8;
    bf16x8 xf[40], W[5];
#define PACK(acc, f) u32x4{cvtpk(fmaxf(acc[(f) & 2][(f) & 1][0], 0.f), fmaxf(acc[(f) & 2][(f) & 1][1], 0.f)), cvtpk(fmaxf(acc[(f) & 2][(f) & 1][2], 0.f), fmaxf(acc[(f) & 2][(f) & 1][3], 0.f)), \
      cvtpk(fmaxf(acc[((f) & 2) + 1][(f) & 1][0], 0.f), fmaxf(acc[((f) & 2) + 1][(f) & 1][1], 0.f)), cvtpk(fmaxf(acc[((f) & 2) + 1][(f) & 1][2], 0.f), fmaxf(acc[((f) & 2) + 1][(f) & 1][3], 0.f))}
#define PUB(acc, f, half) __builtin_amdgcn_raw_buffer_store_b128(PACK(acc, f), rws, lane * 16, hdst + ((half) * 4 + (f)) * 1024, 16)
    WAITV(0); BAR();
    LSTAMP(1); CSTAMP(12);
    auto rd = [&](auto jc) {
      constexpr int j = decltype(jc)::value; constexpr Op o = op1(j);
      if constexpr (o.first && o.ks == 0 && o.v > 0) BAR();
      if constexpr (j == NOP1 || j == NOP1 + 16) BAR();
      if constexpr (o.first && !(o.ks & 1)) {
        xf[o.v * 4 + o.ks] = lds128(xa[o.ks >> 1] + (o.v & 3) * 24576);
        xf[o.v * 4 + o.ks + 1] = lds128(xa[o.ks >> 1] + (o.v & 3) * 24576 + 2048);
      }
      constexpr int off = (o.half ? w1bslot(o.v) * 8192 : 65536 + (o.v & 3) * 24576 + 16384) + (o.ks >> 1) * 4096, fi = (o.ks & 1) * 2 + o.c;
      W[j % 5] = tr2(wa[fi] + off, wa[fi] + off + 512);
    };
    sfor<4>([&](auto jc) { SB(); rd(jc); });
    sfor<160>([&](auto jc) {
      constexpr int j = decltype(jc)::value; constexpr Op o = op1(j);
      SB();
      constexpr int fi = (o.ks & 1) * 2 + o.c, xi = o.v * 4 + (o.ks & 2);
      if constexpr (o.half) ab[fi][0] = MFMA(W[j % 5], xf[xi], ab[fi][0]); else aa[fi][0] = MFMA(W[j % 5], xf[xi], aa[fi][0]);
      SB();
      if constexpr (j + 4 < 160) rd(std::integral_constant<int, j + 4>{});
      if constexpr (o.half) ab[fi][1] = MFMA(W[j % 5], xf[xi + 1], ab[fi][1]); else aa[fi][1] = MFMA(W[j % 5], xf[xi + 1], aa[fi][1]);
      __builtin_amdgcn_sched_group_barrier(0x100, 1, 0); __builtin_amdgcn_sched_group_barrier(0x8, 1, 0); __builtin_amdgcn_sched_group_barrier(0x100, 3, 0);
      SB();
      if constexpr (j == 8) __builtin_amdgcn_raw_buffer_store_b64(__builtin_bit_cast(u32x2, tag), rws, (w == 0 && lane == 0) ? (int)(WS_ACK + b * 8) : OOB, 0, 16);
      if constexpr (j > NOP1 && j <= NOP1 + 8 && ((j - NOP1) & 1) == 0) PUB(aa, (j - NOP1) / 2 - 1, 0);
      if constexpr (j == NOP1) { LSTAMP(7); }
      if constexpr (j == FLAGA_AT) { WAITV(0); FLAG(0); LSTAMP(8); }
    });
    SB();
    LSTAMP(2); CSTAMP(13);
    hb[0] = PACK(ab, 0); hb[1] = PACK(ab, 1); hb[2] = PACK(ab, 2); hb[3] = PACK(ab, 3);
    if (FLAGA_AT >= 160) { WAITV(0); FLAG(0); LSTAMP(8); }
    if (!has2) {
      PUBB(0); PUBB(1); PUBB(2); PUBB(3);
      WAITV(0);
      if (ct < 13) FLAG(1);
      DUMP();
      return;
    }
  }
  PUBB(0); PUBB(1); PUBB(2); PUBB(3);
  BAR();
#pragma unroll
  for (int n = 0; n < 20; ++n) wq[n] = RW2(14 + n);
  WAITL0(); BAR();
#pragma unroll
  for (int n = 20; n < 26; ++n) wq[n] = RW2(14 + n);
  WAITV(0);
  __builtin_amdgcn_raw_buffer_store_b64(__builtin_bit_cast(u32x2, tag), rws, fob, 8192, 16);
  LSTAMP(3);
  WAITL0();
  f32x4 a2[3][2][2];
#pragma unroll
  for (int c = 0; c < 12; ++c) a2[c >> 2][(c >> 1) & 1][c & 1] = f32x4{0.f, 0.f, 0.f, 0.f};
  unsigned ha[3][2];
#pragma unroll
  for (int c = 0; c < 6; ++c) ha[c >> 1][c & 1] = lds0 + 65536 + kh * 2048 + lane * 16 + (kh ? 2 - (c >> 1) : (c >> 1)) * 4096 + ((c & 1) ^ kh) * 1024;
  bf16x8 H[3][3], Wr[2][2];
#define RH(m, n) H[(n) % 3][m] = lds128(ha[m][(n) & 1] + (((n) >> 2) & 3) * 24576 + (((n) >> 1) & 1) * 12288)
  BAR();
  LSTAMP(4); CSTAMP(14);
  SB(); Wr[0][0] = RW2(0); SB(); RH(0, 0); SB(); Wr[0][1] = RW2(1); SB(); RH(1, 0); SB(); RH(2, 0); SB(); RH(0, 1); SB(); RH(1, 1); SB(); RH(2, 1); SB();
  sfor<54>([&](auto nc) {
    constexpr int n = decltype(nc)::value, m = n + 2, s = n % 3, e = n & 1, u = n >> 1;
    constexpr bool reg = u >= 7 && u < 20;
    const bf16x8 w0 = reg ? wq[reg ? 2 * u - 14 : 0] : Wr[u & 1][0], w1f = reg ? wq[reg ? 2 * u - 13 : 0] : Wr[u & 1][1];
    a2[0][e][0] = MFMA(w0, H[s][0], a2[0][e][0]); a2[0][e][1] = MFMA(w1f, H[s][0], a2[0][e][1]); SB();
    if constexpr (m < 54 && (m & 3) == 0) BAR();
    if constexpr (e == 0 && (m < 14 || (m >= 40 && m < 54))) Wr[(m >> 1) & 1][0] = RW2(m);
    if constexpr (m < 54) RH(0, m);
    SB();
    a2[1][e][0] = MFMA(w0, H[s][1], a2[1][e][0]); a2[1][e][1] = MFMA(w1f, H[s][1], a2[1][e][1]); SB();
    if constexpr (e == 0 && (m < 14 || (m >= 40 && m < 54))) Wr[(m >> 1) & 1][1] = RW2(m + 1);
    if constexpr (m < 54) RH(1, m);
    SB();
    a2[2][e][0] = MFMA(w0, H[s][2], a2[2][e][0]); a2[2][e][1] = MFMA(w1f, H[s][2], a2[2][e][1]); SB();
    if constexpr (m < 54) RH(2, m);
    SB();
  });
  LSTAMP(5); CSTAMP(15);
  {
    const unsigned xb = lds0 + 65536 + 2 * 24576 + nb * 12288 + lane * 16;
#pragma unroll
    for (int t = 0; t < 6; ++t) *(LAS f32x4*)(xb + kh * 6144 + t * 1024) = a2[(5 - (t >> 1)) >> 1][(5 - (t >> 1)) & 1][t & 1];
    __amdgpu_buffer_rsrc_t ro = __builtin_amdgcn_make_buffer_rsrc((void*)out, 0, M_ * D_ * 2, 0x00020000);
    const int ob = (96 * rt + (kh ? 80 : 0) + fr) * 1280 + (64 * nt + 32 * nb + (g4 & 1) * 16 + (g4 >> 1) * 8) * 2;
    WAITL0(); BAR();
    f32x4 o[6];
#pragma unroll
    for (int t = 0; t < 6; ++t) o[t] = *(LAS f32x4*)(xb + (kh ^ 1) * 6144 + t * 1024);
#pragma unroll
    for (int t = 0; t < 3; ++t) {
      const f32x4 v0 = o[2 * t] + a2[t >> 1][t & 1][0], v1 = o[2 * t + 1] + a2[t >> 1][t & 1][1];
      auto s0 = __builtin_amdgcn_permlane16_swap(cvtpk(v0[0], v0[1]), cvtpk(v1[0], v1[1]), false, false);
      auto s1 = __builtin_amdgcn_permlane16_swap(cvtpk(v0[2], v0[3]), cvtpk(v1[2], v1[3]), false, false);
      u32x4 ov = {s0[0], s1[0], s0[1], s1[1]};
      __builtin_amdgcn_raw_buffer_store_b128(ov, ro, ob + (kh ? -t : t) * (16 * 1280), 0, OAUX);
    }
  }
  LSTAMP(6);
  DUMP();
#endif
}

extern "C" void kernel_launch(void* const* d_in, const int* in_sizes, int n_in, void* d_out, int out_size, void* d_ws,
                              size_t ws_size, hipStream_t stream) {
  (void)hipFuncSetAttribute((const void*)ffn_fused, hipFuncAttributeMaxDynamicSharedMemorySize, LDS_BYTES);
  ffn_fused<<<256, 512, LDS_BYTES, stream>>>((const char*)d_in[0], (const char*)d_in[1], (const char*)d_in[2], (char*)d_out, (char*)d_ws);
}
```

```cpp
#include <hip/hip_runtime.h>
#include <utility>
typedef short s16x4 __attribute__((ext_vector_type(4)));
typedef short s16x8 __attribute__((ext_vector_type(8)));
typedef __bf16 bf16x8 __attribute__((ext_vector_type(8)));
typedef __bf16 bf16x2 __attribute__((ext_vector_type(2)));
typedef float f32x2 __attribute__((ext_vector_type(2)));
typedef float f32x4 __attribute__((ext_vector_type(4)));
typedef float f32x16 __attribute__((ext_vector_type(16)));
typedef unsigned u32x2 __attribute__((ext_vector_type(2)));
typedef unsigned u32x4 __attribute__((ext_vector_type(4)));
typedef unsigned long long u64;
#define LAS __attribute__((address_space(3)))
#define GAS __attribute__((address_space(1)))
#define DI __device__ __forceinline__

constexpr int M_ = 2048, D_ = 640, F_ = 1728;
constexpr unsigned LDS_BYTES = 163840;
constexpr unsigned WS_EPOCH = 0;
constexpr unsigned WS_ACK = 4096;
constexpr unsigned WS_HFLAG = 8192;
constexpr unsigned WS_STAMP = 1u << 17;
constexpr unsigned WS_H = 1u << 20;
constexpr unsigned WS_END = WS_H + 64 * 108 * 1024;
constexpr unsigned SPIN_MAX = 1u << 16;
#define FLAGA_AT 136
#define SPRA 1
#define FLAGB_AT 12
#define PSLEEP 1
#define TSPLIT 135
constexpr int CODE_BYTES = 21504;
#define OAUX 16
#define XSTAG 1
constexpr int OOB = 0x7ffffff0;

#define WAITV(n) asm volatile("s_waitcnt vmcnt(" #n ")" ::: "memory")
#define WAITL0() asm volatile("s_waitcnt lgkmcnt(0)" ::: "memory")
#define BAR() __builtin_amdgcn_s_barrier()
#define SB() __builtin_amdgcn_sched_barrier(0)
#define LSTAMP(k) do {} while (0)
#define CSTAMP(k) do {} while (0)
#define DUMP() do {} while (0)

template <class F, int... Is> DI void sfor_(F&& f, std::integer_sequence<int, Is...>) { (f(std::integral_constant<int, Is>{}), ...); }
template <int N, class F> DI void sfor(F&& f) { sfor_(f, std::make_integer_sequence<int, N>{}); }
DI unsigned cvtpk(float lo, float hi) { f32x2 v = {lo, hi}; bf16x2 r = __builtin_convertvector(v, bf16x2); return __builtin_bit_cast(unsigned, r); }
DI unsigned relupk(float lo, float hi) {
  typedef short s16x2 __attribute__((ext_vector_type(2)));
  const s16x2 v = __builtin_bit_cast(s16x2, cvtpk(lo, hi)), z = {0, 0};
  return __builtin_bit_cast(unsigned, __builtin_elementwise_max(v, z));
}
DI bf16x8 tr2(unsigned a0, unsigned a1) {
  s16x4 lo = __builtin_amdgcn_ds_read_tr16_b64_v4i16((LAS s16x4*)a0);
  s16x4 hi = __builtin_amdgcn_ds_read_tr16_b64_v4i16((LAS s16x4*)a1);
  s16x8 v = __builtin_shufflevector(lo, hi, 0, 1, 2, 3, 4, 5, 6, 7);
  return __builtin_bit_cast(bf16x8, v);
}
DI bf16x8 lds128(unsigned a) { return *(LAS bf16x8*)a; }
#define MFMA(a, b, c) __builtin_amdgcn_mfma_f32_16x16x32_bf16((a), (b), (c), 0, 0, 0)
#define RLX_AGENT __ATOMIC_RELAXED, __HIP_MEMORY_SCOPE_AGENT
#define DMA(rsrc, ldsoff, voff, soff, aux) __builtin_amdgcn_raw_ptr_buffer_load_lds(rsrc, (LAS void*)(smem + (ldsoff)), 16, voff, soff, 0, aux)
__host__ __device__ constexpr int unit(int p) { return p < 14 ? 2 * p : 2 * (p - 14) + 1; }
#define NBV 3
constexpr int NB = NBV, NOP1 = 80 + 8 * NB;
struct Op { int v, ks, half, c, first; };
__host__ __device__ constexpr Op op1(int j) {
  if (j >= NOP1) { const int r = j - NOP1; return Op{NB + (r >> 3), (r >> 1) & 3, 1, r & 1, 0}; }
  if (j < 16) return Op{j >> 3, (j >> 1) & 3, 0, j & 1, !(j & 1)};
  if (j < 16 + 16 * NB) { const int r = j - 16, st = 2 + (r >> 4), h = (r >> 1) & 1; return Op{h ? st - 2 : st, (r >> 2) & 3, h, r & 1, (r & 3) == 0}; }
  const int r = j - 16 - 16 * NB; return Op{2 + NB + (r >> 3), (r >> 1) & 3, 0, r & 1, !(r & 1)};
}
__host__ __device__ constexpr int w2slot(int p) { return p < 7 ? p : p < 11 ? p + 9 : p < 17 ? p - 3 : p == 17 ? 7 : p < 20 ? p - 4 : p - 20; }
__host__ __device__ constexpr int w1bslot(int hv) { return hv < 8 ? hv : 6 + hv; }

DI void helper(int b, int lane, int wid, const char* w2, char* ws, char* smem) {
#if defined(__HIP_DEVICE_COMPILE__)
  const int xr = (b & 7) >> 1, xc = b & 1, hw = wid * 4 + ((b >> 3) - 28);
  LAS void* sink = (LAS void*)(smem + wid * 256);
  if (hw < 3) {
    u64 pc; unsigned po;
    asm volatile("s_getpc_b64 %0\n.Lpc%=:\n\ts_mov_b32 %1, .Lpc%=-ffn_fused" : "=s"(pc), "=s"(po));
    __amdgpu_buffer_rsrc_t rc = __builtin_amdgcn_make_buffer_rsrc((void*)(pc - po), 0, CODE_BYTES, 0x00020000);
    __builtin_amdgcn_raw_ptr_buffer_load_lds(rc, sink, 4, (hw * 64 + lane) * 128, 0, 0, 0);
  }
  __amdgpu_buffer_rsrc_t r2 = __builtin_amdgcn_make_buffer_rsrc((void*)w2, 0, F_ * D_ * 2, 0x00020000);
#pragma unroll 1
  for (int t = hw; t < 140; t += 32) {
    const int u0 = t / 5, c = t - u0 * 5, uo = (u0 & ~3) | ((u0 + XSTAG * xr) & 3);
    const int u = unit(uo < 10 ? uo + 7 : uo < 17 ? uo - 10 : uo);
    __builtin_amdgcn_raw_ptr_buffer_load_lds(r2, sink, 4, uo < 27 ? lane * 1280 : OOB, min(u, 26) * (64 * 1280) + xc * 640 + c * 128, 0, 0);
  }
  if (wid != 7) { WAITV(0); return; }
  const u64 tag = __hip_atomic_load((GAS u64*)(ws + WS_EPOCH), RLX_AGENT) + 1;
  if (lane == 0) __hip_atomic_store((GAS u64*)(ws + WS_ACK + b * 8), tag, RLX_AGENT);
  if (b == 255) {
    for (unsigned spin = 0; spin < SPIN_MAX; ++spin) {
      bool ok = true;
#pragma unroll
      for (int q = 0; q < 4; ++q) ok &= __hip_atomic_load((GAS u64*)(ws + WS_ACK + (q * 64 + lane) * 8), RLX_AGENT) == tag;
      if (__all(ok)) break;
      __builtin_amdgcn_s_sleep(8);
    }
    if (lane == 0) __hip_atomic_store((GAS u64*)(ws + WS_EPOCH), tag, RLX_AGENT);
  }
  WAITV(0);
#endif
}

extern "C" __global__ __launch_bounds__(512, 2) void ffn_fused(const char* __restrict__ x, const char* __restrict__ w1, const char* __restrict__ w2,
                                                    char* __restrict__ out, char* __restrict__ ws) {
#if defined(__HIP_DEVICE_COMPILE__)
  extern __shared__ __attribute__((aligned(16))) char smem[];
  const int b = blockIdx.x, xr = (b & 7) >> 1, xc = b & 1, kk = b >> 3;
  const int lane = threadIdx.x & 63, wid = __builtin_amdgcn_readfirstlane(threadIdx.x >> 6);
  if (__builtin_expect(kk >= 28, 0)) { helper(b, lane, wid, w2, ws, smem); return; }
  const int i = 4 * xr + (kk & 3), cl = kk >> 2, ct = 7 * xc + cl;
  const int L = wid & 3;
  const int wsw = ((lane & 7) ^ (((lane >> 4) & 1) << 1)) << 4;
  int krot = ((kk ^ cl) & 1) * 5 + xr + xc * 2; krot -= krot >= 10 ? 10 : 0;
  const int vx0 = (128 * i + 32 * L + (lane >> 3)) * 1280 + (((lane & 7) ^ (lane >> 4)) << 4), vx1 = (vx0 + 8 * 1280) ^ 64;
  const int v1 = ((lane >> 3) + 16 * L) * 3456 + ct * 256 + wsw;
  __amdgpu_buffer_rsrc_t rx = __builtin_amdgcn_make_buffer_rsrc((void*)x, 0, M_ * D_ * 2, 0x00020000);
  __amdgpu_buffer_rsrc_t r1 = __builtin_amdgcn_make_buffer_rsrc((void*)w1, 0, D_ * F_ * 2, 0x00020000);
  int kb = krot + (wid >> 2); kb -= kb >= 10 ? 10 : 0;
  unsigned base = 65536u + (wid >> 2) * 24576u;
  auto g1 = [&]() {
    DMA(rx, base + L * 4096, vx0, kb * 128, 0);
    DMA(rx, base + L * 4096 + 1024, vx1, kb * 128, 0);
    DMA(rx, base + L * 4096 + 2048, vx0, kb * 128 + 16 * 1280, 0);
    DMA(rx, base + L * 4096 + 3072, vx1, kb * 128 + 16 * 1280, 0);
    DMA(r1, base + 16384 + L * 2048, v1, kb * (64 * 3456), 0);
    DMA(r1, base + 16384 + L * 2048 + 1024, v1 ^ 64, kb * (64 * 3456) + 8 * 3456, 0);
    kb = (kb == 9) ? 0 : kb + 1;
    base = (base == 65536u + 3 * 24576u) ? 65536u : base + 24576u;
  };
  g1();
  SB();
  asm volatile("" ::"s"(w2), "s"(out), "s"(ws));
  const int t2 = (xr >> 1) * 55 + (xr & 1) * 28 + kk;
  const bool has2 = kk < 28 - (xr & 1);
  const int rt = t2 / 5, nl = t2 - rt * 5, nt = 5 * xc + nl;
  LSTAMP(0);
  __amdgpu_buffer_rsrc_t rws = __builtin_amdgcn_make_buffer_rsrc((void*)ws, 0, WS_END, 0x00020000);

  if (__builtin_expect(wid >= 4, 1)) {
    u64 tag;
    asm volatile("global_load_dwordx2 %0, %1, %2 sc1" : "=v"(tag) : "v"(0), "s"(ws + WS_EPOCH) : "memory");
    __amdgpu_buffer_rsrc_t r2 = __builtin_amdgcn_make_buffer_rsrc((void*)w2, 0, F_ * D_ * 2, 0x00020000);
    const int v2 = has2 ? ((lane >> 3) + 16 * L) * 1280 + nt * 128 + (((lane & 7) ^ ((lane >> 4) << 1)) << 4) : OOB;
    auto wu = [&](int p) {
      const int so = unit(p) * (64 * 1280);
      DMA(r2, w2slot(p) * 8192 + L * 2048, v2, so, 0);
      DMA(r2, w2slot(p) * 8192 + L * 2048 + 1024, v2, so + 8 * 1280, 0);
    };
    const int v1b = ct < 13 ? v1 + 128 : OOB;
    int kc = krot;
    auto g1b = [&](int hv) {
      DMA(r1, w1bslot(hv) * 8192 + L * 2048, v1b, kc * (64 * 3456), 0);
      DMA(r1, w1bslot(hv) * 8192 + L * 2048 + 1024, v1b ^ 64, kc * (64 * 3456) + 8 * 3456, 0);
      kc = (kc == 9) ? 0 : kc + 1;
    };
    g1b(0); g1b(1); g1b(2);
    BAR(); g1();
    WAITV(12); BAR(); g1();
#pragma unroll 1
    for (int v = 2; v < 8; ++v) { WAITV(6); BAR(); g1(); }
    WAITV(6); BAR(); g1b(3); g1b(4); g1b(5); g1b(6);
    WAITV(8); BAR(); g1b(7); g1b(8);
    LSTAMP(1);
    WAITV(8); BAR(); g1b(9); wu(7); wu(8); wu(9);
    WAITV(6); BAR();
    wu(10); wu(11); wu(12); wu(13); wu(14); wu(15);
    BAR();
    if (!has2) { WAITV(0); DUMP(); return; }
    wu(16); wu(0); wu(1); wu(2); wu(3); wu(4); wu(5); wu(6);
    WAITV(14); BAR();
    LSTAMP(2);
    asm volatile("" : "+v"(tag));
    tag += 1;
    const int mb0 = 3 * rt;
    const bool fl0 = lane < 48 && (lane & 15) < 14, fl1 = lane < 48 && (lane & 15) < 13;
    const GAS u64* fp = (const GAS u64*)(ws + WS_HFLAG + min(mb0 + min(lane >> 4, 2), 63) * 128 + (lane & 15) * 8);
    u64 fs;
    auto poll = [&](int half) {
      if (!__all(fs == tag || !(half ? fl1 : fl0))) {
        for (unsigned spin = 0; spin < SPIN_MAX; ++spin) {
          const u64 f = __hip_atomic_load(fp + half * 1024, RLX_AGENT);
          if (__all(f == tag || !(half ? fl1 : fl0))) break;
          __builtin_amdgcn_s_sleep(PSLEEP);
        }
      }
      __builtin_amdgcn_fence(__ATOMIC_ACQUIRE, "wavefront");
    };
    asm volatile("global_load_dwordx2 %0, %1, off sc1" : "=v"(fs) : "v"(fp) : "memory");
    wu(17); wu(18); wu(19);
    asm volatile("s_waitcnt vmcnt(6)" : "+v"(fs)::"memory");
    BAR();
    LSTAMP(7);
    poll(0);
    LSTAMP(3);
    const unsigned hs0 = WS_H + (mb0 * 108 + L) * 1024, hs1 = WS_H + (min(mb0 + 1, 63) * 108 + L) * 1024, hs2 = WS_H + (min(mb0 + 2, 63) * 108 + L) * 1024;
    unsigned hbase = 65536u + L * 1024u;
    auto hs = [&](int g) {
#pragma unroll
      for (int e = 0; e < 2; ++e) {
        const unsigned o = (unsigned)unit(min(2 * g + e, 26)) * 4096u;
        const int vv = (2 * g + e < 27) ? lane * 16 : OOB;
        DMA(rws, hbase + e * 12288, vv, hs0 + o, 16);
        DMA(rws, hbase + e * 12288 + 4096, vv, hs1 + o, 16);
        DMA(rws, hbase + e * 12288 + 8192, vv, hs2 + o, 16);
      }
      hbase = (hbase >= 65536u + 3 * 24576u) ? hbase - 3 * 24576u : hbase + 24576u;
    };
    hs(0); hs(1);
    WAITV(12); BAR();
#pragma unroll 1
    for (int g = 0; g < 4; ++g) { WAITV(6); BAR(); hs(g + 2); }
    WAITV(6); BAR();
    asm volatile("global_load_dwordx2 %0, %1, off sc1" : "=v"(fs) : "v"(fp + 1024) : "memory");
    wu(20); wu(21); hs(6);
    asm volatile("s_waitcnt vmcnt(6)" : "+v"(fs)::"memory");
    BAR();
    LSTAMP(5); poll(1); LSTAMP(6);
    wu(22); wu(23); hs(7);
    WAITV(6); BAR(); wu(24); hs(8);
    WAITV(6); BAR(); wu(25); hs(9);
    WAITV(6); BAR(); wu(26); hs(10);
#pragma unroll 1
    for (int g = 9; g < 12; ++g) { WAITV(6); BAR(); hs(g + 2); }
    WAITV(6); BAR();
    WAITV(0); BAR();
    LSTAMP(4);
    DUMP();
    return;
  }
  const unsigned lds0 = (unsigned)(size_t)(LAS char*)smem;
  const int w = wid, fr = lane & 15, g4 = lane >> 4, q4 = fr >> 2, p4 = lane & 3, swb = (q4 >> 1) | ((g4 & 1) << 1);
#define PRIO 3
  __builtin_amdgcn_s_setprio(PRIO);
  const u64 tag = __hip_atomic_load((GAS u64*)(ws + WS_EPOCH), RLX_AGENT) + 1;
  asm volatile("" ::"v"(tag));
  const int fo = lane == 0 ? (int)(WS_HFLAG + (i * 4 + w) * 128 + ct * 8) : OOB;
#define FLAG(half) __builtin_amdgcn_raw_buffer_store_b64(__builtin_bit_cast(u32x2, tag), rws, fo, (half) * 8192, 16)
  const int fob = (ct < 13) ? fo : OOB;
  const int kh = w >> 1, nb = w & 1;
  bf16x8 wq[26];
  const unsigned w2a = lds0 + (32 * kh + 4 * g4 + q4) * 128 + (((((2 * nb) ^ swb) << 1) | (p4 >> 1)) << 4) + (p4 & 1) * 8, w2b = w2a ^ 32;
#define RW2(n) tr2((((n) & 1) ? w2b : w2a) + w2slot((n) >> 1) * 8192, (((n) & 1) ? w2b : w2a) + w2slot((n) >> 1) * 8192 + 2048)
  const unsigned hdst = WS_H + ((i * 4 + w) * 108 + ct * 8) * 1024;
  const int hvo = (ct < 13) ? lane * 16 : OOB;
  u32x4 hb[4] = {};
#define PUBB(f) __builtin_amdgcn_raw_buffer_store_b128(hb[f], rws, hvo, hdst + (4 + (f)) * 1024, 16)
  {
    f32x4 aa[4][2], ab[4][2];
#pragma unroll
    for (int c = 0; c < 8; ++c) { aa[c >> 1][c & 1] = f32x4{0.f, 0.f, 0.f, 0.f}; ab[c >> 1][c & 1] = f32x4{0.f, 0.f, 0.f, 0.f}; }
    unsigned xa[2], wa[4];
#pragma unroll
    for (int ks = 0; ks < 2; ++ks) xa[ks] = lds0 + 65536 + (32 * w + fr) * 128 + (((4 * ks + g4) ^ (fr >> 1)) << 4);
#pragma unroll
    for (int c = 0; c < 4; ++c) wa[c] = lds0 + (8 * g4 + q4) * 128 + ((((c ^ swb) << 1) | (p4 >> 1)) << 4) + (p4 & 1) * 8;
    bf16x8 xf[40], W[5];
#define PACK(acc, f) u32x4{relupk(acc[(f) & 2][(f) & 1][0], acc[(f) & 2][(f) & 1][1]), relupk(acc[(f) & 2][(f) & 1][2], acc[(f) & 2][(f) & 1][3]), \
      relupk(acc[((f) & 2) + 1][(f) & 1][0], acc[((f) & 2) + 1][(f) & 1][1]), relupk(acc[((f) & 2) + 1][(f) & 1][2], acc[((f) & 2) + 1][(f) & 1][3])}
#define PUB(acc, f, half) __builtin_amdgcn_raw_buffer_store_b128(PACK(acc, f), rws, lane * 16, hdst + ((half) * 4 + (f)) * 1024, 16)
    WAITV(0); BAR();
    LSTAMP(1); CSTAMP(12);
    auto rd = [&](auto jc) {
      constexpr int j = decltype(jc)::value; constexpr Op o = op1(j);
      if constexpr (o.first && o.ks == 0 && o.v > 0) BAR();
      if constexpr (j == NOP1 || j == NOP1 + 16 || j == NOP1 + 40) BAR();
      if constexpr (o.first && !(o.ks & 1)) {
        xf[o.v * 4 + o.ks] = lds128(xa[o.ks >> 1] + (o.v & 3) * 24576);
        xf[o.v * 4 + o.ks + 1] = lds128(xa[o.ks >> 1] + (o.v & 3) * 24576 + 2048);
      }
      constexpr int off = (o.half ? w1bslot(o.v) * 8192 : 65536 + (o.v & 3) * 24576 + 16384) + (o.ks >> 1) * 4096, fi = (o.ks & 1) * 2 + o.c;
      W[j % 5] = tr2(wa[fi] + off, wa[fi] + off + 512);
    };
    sfor<4>([&](auto jc) { SB(); rd(jc); });
    sfor<160>([&](auto jc) {
      constexpr int j = decltype(jc)::value; constexpr Op o = op1(j);
      SB();
      constexpr int fi = (o.ks & 1) * 2 + o.c, xi = o.v * 4 + (o.ks & 2);
      if constexpr (o.half) ab[fi][0] = MFMA(W[j % 5], xf[xi], ab[fi][0]); else aa[fi][0] = MFMA(W[j % 5], xf[xi], aa[fi][0]);
      SB();
      if constexpr (j + 4 < 160) rd(std::integral_constant<int, j + 4>{});
      if constexpr (o.half) ab[fi][1] = MFMA(W[j % 5], xf[xi + 1], ab[fi][1]); else aa[fi][1] = MFMA(W[j % 5], xf[xi + 1], aa[fi][1]);
      __builtin_amdgcn_sched_group_barrier(0x100, 1, 0); __builtin_amdgcn_sched_group_barrier(0x8, 1, 0); __builtin_amdgcn_sched_group_barrier(0x100, 3, 0);
      SB();
      if constexpr (j == 8) __builtin_amdgcn_raw_buffer_store_b64(__builtin_bit_cast(u32x2, tag), rws, (w == 0 && lane == 0) ? (int)(WS_ACK + b * 8) : OOB, 0, 16);
      if constexpr (j > NOP1 && j <= NOP1 + 8 && ((j - NOP1) & 1) == 0) PUB(aa, (j - NOP1) / 2 - 1, 0);
      if constexpr (j == NOP1) { LSTAMP(7); }
      if constexpr (j == FLAGA_AT) { WAITV(0); FLAG(0); LSTAMP(8); }
    });
    SB();
    LSTAMP(2); CSTAMP(13);
    hb[0] = PACK(ab, 0); hb[1] = PACK(ab, 1); hb[2] = PACK(ab, 2); hb[3] = PACK(ab, 3);
    if (FLAGA_AT >= 160) { WAITV(0); FLAG(0); LSTAMP(8); }
    if (!has2) {
      PUBB(0); PUBB(1); PUBB(2); PUBB(3);
      WAITV(0);
      if (ct < 13) FLAG(1);
      DUMP();
      return;
    }
  }
  PUBB(0); PUBB(1); PUBB(2); PUBB(3);
  BAR();
#pragma unroll
  for (int n = 0; n < 20; ++n) wq[n] = RW2(14 + n);
  WAITL0(); BAR();
  WAITV(0);
  __builtin_amdgcn_raw_buffer_store_b64(__builtin_bit_cast(u32x2, tag), rws, fob, 8192, 16);
  LSTAMP(3);
  BAR();
#pragma unroll
  for (int n = 20; n < 26; ++n) wq[n] = RW2(14 + n);
  WAITL0();
  f32x4 a2[3][2][2];
#pragma unroll
  for (int c = 0; c < 12; ++c) a2[c >> 2][(c >> 1) & 1][c & 1] = f32x4{0.f, 0.f, 0.f, 0.f};
  unsigned ha[3][2];
#pragma unroll
  for (int c = 0; c < 6; ++c) ha[c >> 1][c & 1] = lds0 + 65536 + kh * 2048 + lane * 16 + (kh ? 2 - (c >> 1) : (c >> 1)) * 4096 + ((c & 1) ^ kh) * 1024;
  bf16x8 H[3][3], Wr[2][2];
#define RH(m, n) H[(n) % 3][m] = lds128(ha[m][(n) & 1] + (((n) >> 2) & 3) * 24576 + (((n) >> 1) & 1) * 12288)
  BAR();
  LSTAMP(4); CSTAMP(14);
  SB(); Wr[0][0] = RW2(0); SB(); RH(0, 0); SB(); Wr[0][1] = RW2(1); SB(); RH(1, 0); SB(); RH(2, 0); SB(); RH(0, 1); SB(); RH(1, 1); SB(); RH(2, 1); SB();
  sfor<54>([&](auto nc) {
    constexpr int n = decltype(nc)::value, m = n + 2, s = n % 3, e = n & 1, u = n >> 1;
    constexpr bool reg = u >= 7 && u < 20;
    const bf16x8 w0 = reg ? wq[reg ? 2 * u - 14 : 0] : Wr[u & 1][0], w1f = reg ? wq[reg ? 2 * u - 13 : 0] : Wr[u & 1][1];
    a2[0][e][0] = MFMA(w0, H[s][0], a2[0][e][0]); a2[0][e][1] = MFMA(w1f, H[s][0], a2[0][e][1]); SB();
    if constexpr (m < 54 && (m & 3) == 0) BAR();
    if constexpr (e == 0 && (m < 14 || (m >= 40 && m < 54))) Wr[(m >> 1) & 1][0] = RW2(m);
    if constexpr (m < 54) RH(0, m);
    SB();
    a2[1][e][0] = MFMA(w0, H[s][1], a2[1][e][0]); a2[1][e][1] = MFMA(w1f, H[s][1], a2[1][e][1]); SB();
    if constexpr (e == 0 && (m < 14 || (m >= 40 && m < 54))) Wr[(m >> 1) & 1][1] = RW2(m + 1);
    if constexpr (m < 54) RH(1, m);
    SB();
    a2[2][e][0] = MFMA(w0, H[s][2], a2[2][e][0]); a2[2][e][1] = MFMA(w1f, H[s][2], a2[2][e][1]); SB();
    if constexpr (m < 54) RH(2, m);
    SB();
  });
  LSTAMP(5); CSTAMP(15);
  {
    const unsigned xb = lds0 + 65536 + 2 * 24576 + nb * 12288 + lane * 16;
#pragma unroll
    for (int t = 0; t < 6; ++t) *(LAS f32x4*)(xb + kh * 6144 + t * 1024) = a2[(5 - (t >> 1)) >> 1][(5 - (t >> 1)) & 1][t & 1];
    __amdgpu_buffer_rsrc_t ro = __builtin_amdgcn_make_buffer_rsrc((void*)out, 0, M_ * D_ * 2, 0x00020000);
    const int ob = (96 * rt + (kh ? 80 : 0) + fr) * 1280 + (64 * nt + 32 * nb + (g4 & 1) * 16 + (g4 >> 1) * 8) * 2;
    WAITL0(); BAR();
    f32x4 o[6];
#pragma unroll
    for (int t = 0; t < 6; ++t) o[t] = *(LAS f32x4*)(xb + (kh ^ 1) * 6144 + t * 1024);
#pragma unroll
    for (int t = 0; t < 3; ++t) {
      const f32x4 v0 = o[2 * t] + a2[t >> 1][t & 1][0], v1 = o[2 * t + 1] + a2[t >> 1][t & 1][1];
      auto s0 = __builtin_amdgcn_permlane16_swap(cvtpk(v0[0], v0[1]), cvtpk(v1[0], v1[1]), false, false);
      auto s1 = __builtin_amdgcn_permlane16_swap(cvtpk(v0[2], v0[3]), cvtpk(v1[2], v1[3]), false, false);
      u32x4 ov = {s0[0], s1[0], s0[1], s1[1]};
      __builtin_amdgcn_raw_buffer_store_b128(ov, ro, ob + (kh ? -t : t) * (16 * 1280), 0, OAUX);
    }
  }
  LSTAMP(6);
  DUMP();
#endif
}

extern "C" void kernel_launch(void* const* d_in, const int* in_sizes, int n_in, void* d_out, int out_size, void* d_ws,
                              size_t ws_size, hipStream_t stream) {
  (void)hipFuncSetAttribute((const void*)ffn_fused, hipFuncAttributeMaxDynamicSharedMemorySize, LDS_BYTES);
  ffn_fused<<<256, 512, LDS_BYTES, stream>>>((const char*)d_in[0], (const char*)d_in[1], (const char*)d_in[2], (char*)d_out, (char*)d_ws);
}
```

```cpp
#include <hip/hip_runtime.h>
#include <utility>
typedef short s16x4 __attribute__((ext_vector_type(4)));
typedef short s16x8 __attribute__((ext_vector_type(8)));
typedef __bf16 bf16x8 __attribute__((ext_vector_type(8)));
typedef __bf16 bf16x2 __attribute__((ext_vector_type(2)));
typedef float f32x2 __attribute__((ext_vector_type(2)));
typedef float f32x4 __attribute__((ext_vector_type(4)));
typedef float f32x16 __attribute__((ext_vector_type(16)));
typedef unsigned u32x2 __attribute__((ext_vector_type(2)));
typedef unsigned u32x4 __attribute__((ext_vector_type(4)));
typedef unsigned long long u64;
#define LAS __attribute__((address_space(3)))
#define GAS __attribute__((address_space(1)))
#define DI __device__ __forceinline__

constexpr int M_ = 2048, D_ = 640, F_ = 1728;
constexpr unsigned LDS_BYTES = 163840;
constexpr unsigned WS_EPOCH = 0;
constexpr unsigned WS_ACK = 4096;
constexpr unsigned WS_HFLAG = 8192;
constexpr unsigned WS_STAMP = 1u << 17;
constexpr unsigned WS_H = 1u << 20;
constexpr unsigned WS_END = WS_H + 64 * 108 * 1024;
constexpr unsigned SPIN_MAX = 1u << 16;
#define FLAGA_AT 136
#define SPRA 1
#define FLAGB_AT 12
#define PSLEEP 1
#define TSPLIT 135
constexpr int CODE_BYTES = 20480;
#define OAUX 16
#define XSTAG 1
#define AX 0
#define A1 0
#define AT 0
#define A2 0
#define AXL 0
#define A1L 0
constexpr int OOB = 0x7ffffff0;

#define WAITV(n) asm volatile("s_waitcnt vmcnt(" #n ")" ::: "memory")
#define WAITL0() asm volatile("s_waitcnt lgkmcnt(0)" ::: "memory")
#define BAR() __builtin_amdgcn_s_barrier()
#define SB() __builtin_amdgcn_sched_barrier(0)
#define LSTAMP(k) do {} while (0)
#define CSTAMP(k) do {} while (0)
#define DUMP() do {} while (0)

template <class F, int... Is> DI void sfor_(F&& f, std::integer_sequence<int, Is...>) { (f(std::integral_constant<int, Is>{}), ...); }
template <int N, class F> DI void sfor(F&& f) { sfor_(f, std::make_integer_sequence<int, N>{}); }
DI unsigned cvtpk(float lo, float hi) { f32x2 v = {lo, hi}; bf16x2 r = __builtin_convertvector(v, bf16x2); return __builtin_bit_cast(unsigned, r); }
DI unsigned relupk(float lo, float hi) {
  typedef short s16x2 __attribute__((ext_vector_type(2)));
  const s16x2 v = __builtin_bit_cast(s16x2, cvtpk(lo, hi)), z = {0, 0};
  return __builtin_bit_cast(unsigned, __builtin_elementwise_max(v, z));
}
DI bf16x8 tr2(unsigned a0, unsigned a1) {
  s16x4 lo = __builtin_amdgcn_ds_read_tr16_b64_v4i16((LAS s16x4*)a0);
  s16x4 hi = __builtin_amdgcn_ds_read_tr16_b64_v4i16((LAS s16x4*)a1);
  s16x8 v = __builtin_shufflevector(lo, hi, 0, 1, 2, 3, 4, 5, 6, 7);
  return __builtin_bit_cast(bf16x8, v);
}
DI bf16x8 lds128(unsigned a) { return *(LAS bf16x8*)a; }
#define MMA(first, c, a, b) do { if constexpr (first) asm volatile("v_mfma_f32_16x16x32_bf16 %0, %1, %2, 0" : "=&v"(c) : "v"(a), "v"(b)); \
    else asm volatile("v_mfma_f32_16x16x32_bf16 %0, %1, %2, %0" : "+v"(c) : "v"(a), "v"(b)); } while (0)
#define MFMA_DRAIN() asm volatile("s_nop 7\n\ts_nop 1" ::: "memory")
DI s16x4 tr1(unsigned a) { return __builtin_amdgcn_ds_read_tr16_b64_v4i16((LAS s16x4*)a); }
#define RLX_AGENT __ATOMIC_RELAXED, __HIP_MEMORY_SCOPE_AGENT
#define DMA(rsrc, ldsoff, voff, soff, aux) __builtin_amdgcn_raw_ptr_buffer_load_lds(rsrc, (LAS void*)(smem + (ldsoff)), 16, voff, soff, 0, aux)
__host__ __device__ constexpr int unit(int p) { return p < 14 ? 2 * p : 2 * (p - 14) + 1; }
#define NBV 3
constexpr int NB = NBV, NOP1 = 80 + 8 * NB;
struct Op { int v, ks, half, c, first; };
__host__ __device__ constexpr Op op1(int j) {
  if (j >= NOP1) { const int r = j - NOP1; return Op{NB + (r >> 3), (r >> 1) & 3, 1, r & 1, 0}; }
  if (j < 16) return Op{j >> 3, (j >> 1) & 3, 0, j & 1, !(j & 1)};
  if (j < 16 + 16 * NB) { const int r = j - 16, st = 2 + (r >> 4), h = (r >> 1) & 1; return Op{h ? st - 2 : st, (r >> 2) & 3, h, r & 1, (r & 3) == 0}; }
  const int r = j - 16 - 16 * NB; return Op{2 + NB + (r >> 3), (r >> 1) & 3, 0, r & 1, !(r & 1)};
}
__host__ __device__ constexpr int w2slot(int p) { return p < 7 ? p : p < 11 ? p + 9 : p < 17 ? p - 3 : p == 17 ? 7 : p < 20 ? p - 4 : p - 20; }
__host__ __device__ constexpr int w1bslot(int hv) { return hv < 8 ? hv : 6 + hv; }

DI void helper(int b, int lane, int wid, const char* w2, char* ws, char* smem) {
#if defined(__HIP_DEVICE_COMPILE__)
  const int xr = (b & 7) >> 1, xc = b & 1, hw = wid * 4 + ((b >> 3) - 28);
  LAS void* sink = (LAS void*)(smem + wid * 256);
  if (hw < 3) {
    u64 pc; unsigned po;
    asm volatile("s_getpc_b64 %0\n.Lpc%=:\n\ts_mov_b32 %1, .Lpc%=-ffn_fused" : "=s"(pc), "=s"(po));
    __amdgpu_buffer_rsrc_t rc = __builtin_amdgcn_make_buffer_rsrc((void*)(pc - po), 0, CODE_BYTES, 0x00020000);
    __builtin_amdgcn_raw_ptr_buffer_load_lds(rc, sink, 4, (hw * 64 + lane) * 128, 0, 0, 0);
  }
  __amdgpu_buffer_rsrc_t r2 = __builtin_amdgcn_make_buffer_rsrc((void*)w2, 0, F_ * D_ * 2, 0x00020000);
#pragma unroll 1
  for (int t = hw; t < 140; t += 32) {
    const int u0 = t / 5, c = t - u0 * 5, uo = (u0 & ~3) | ((u0 + XSTAG * xr) & 3);
    const int u = unit(uo < 10 ? uo + 7 : uo < 17 ? uo - 10 : uo);
    __builtin_amdgcn_raw_ptr_buffer_load_lds(r2, sink, 4, uo < 27 ? lane * 1280 : OOB, min(u, 26) * (64 * 1280) + xc * 640 + c * 128, 0, AT);
  }
  if (wid != 7) { WAITV(0); return; }
  const u64 tag = __hip_atomic_load((GAS u64*)(ws + WS_EPOCH), RLX_AGENT) + 1;
  if (lane == 0) __hip_atomic_store((GAS u64*)(ws + WS_ACK + b * 8), tag, RLX_AGENT);
  if (b == 255) {
    for (unsigned spin = 0; spin < SPIN_MAX; ++spin) {
      bool ok = true;
#pragma unroll
      for (int q = 0; q < 4; ++q) ok &= __hip_atomic_load((GAS u64*)(ws + WS_ACK + (q * 64 + lane) * 8), RLX_AGENT) == tag;
      if (__all(ok)) break;
      __builtin_amdgcn_s_sleep(8);
    }
    if (lane == 0) __hip_atomic_store((GAS u64*)(ws + WS_EPOCH), tag, RLX_AGENT);
  }
  WAITV(0);
#endif
}

extern "C" __global__ __launch_bounds__(512, 2) void ffn_fused(const char* __restrict__ x, const char* __restrict__ w1, const char* __restrict__ w2,
                                                    char* __restrict__ out, char* __restrict__ ws) {
#if defined(__HIP_DEVICE_COMPILE__)
  extern __shared__ __attribute__((aligned(16))) char smem[];
  const int b = blockIdx.x, xr = (b & 7) >> 1, xc = b & 1, kk = b >> 3;
  const int lane = threadIdx.x & 63, wid = __builtin_amdgcn_readfirstlane(threadIdx.x >> 6);
  if (__builtin_expect(kk >= 28, 0)) { helper(b, lane, wid, w2, ws, smem); return; }
  const int i = 4 * xr + (kk & 3), cl = kk >> 2, ct = 7 * xc + cl;
  const int L = wid & 3;
  const int wsw = ((lane & 7) ^ (((lane >> 4) & 1) << 1)) << 4;
#define KROT 9
#if KROT == 9
  int krot = ((kk ^ cl) & 1) * 5 + xr + xc * 2; krot -= krot >= 10 ? 10 : 0;
#elif KROT == 1
  int krot = 3 * (kk & 3) + ((cl * 3) >> 1); krot -= krot >= 10 ? 10 : 0;
#elif KROT == 7
  int krot = ((((kk & 3) + cl) & 3) * 5) >> 1;
#elif KROT == 14
  int krot = (((kk & 3) + cl) % 3) * 3 + (xr >> 1) + xc; krot -= krot >= 10 ? 10 : 0;
#elif KROT == 15
  int krot = (((kk & 3) * 5) >> 1) + (xr >> 1); krot -= krot >= 10 ? 10 : 0;
#elif KROT == 16
  int krot = (kk & 1) * 5 + xr; krot -= krot >= 10 ? 10 : 0;
#endif
  const int vx0 = (128 * i + 32 * L + (lane >> 3)) * 1280 + (((lane & 7) ^ (lane >> 4)) << 4), vx1 = (vx0 + 8 * 1280) ^ 64;
  const int v1 = ((lane >> 3) + 16 * L) * 3456 + ct * 256 + wsw;
  __amdgpu_buffer_rsrc_t rx = __builtin_amdgcn_make_buffer_rsrc((void*)x, 0, M_ * D_ * 2, 0x00020000);
  __amdgpu_buffer_rsrc_t r1 = __builtin_amdgcn_make_buffer_rsrc((void*)w1, 0, D_ * F_ * 2, 0x00020000);
  int kb = krot + (wid >> 2); kb -= kb >= 10 ? 10 : 0;
  unsigned base = 65536u + (wid >> 2) * 24576u;
  auto g1 = [&](auto late) {
    constexpr int ax = decltype(late)::value ? AXL : AX, a1 = decltype(late)::value ? A1L : A1;
    DMA(rx, base + L * 4096, vx0, kb * 128, ax);
    DMA(rx, base + L * 4096 + 1024, vx1, kb * 128, ax);
    DMA(rx, base + L * 4096 + 2048, vx0, kb * 128 + 16 * 1280, ax);
    DMA(rx, base + L * 4096 + 3072, vx1, kb * 128 + 16 * 1280, ax);
    DMA(r1, base + 16384 + L * 2048, v1, kb * (64 * 3456), a1);
    DMA(r1, base + 16384 + L * 2048 + 1024, v1 ^ 64, kb * (64 * 3456) + 8 * 3456, a1);
    kb = (kb == 9) ? 0 : kb + 1;
    base = (base == 65536u + 3 * 24576u) ? 65536u : base + 24576u;
  };
  constexpr std::integral_constant<int, 0> EARLY{}; constexpr std::integral_constant<int, 1> LATE{};
  g1(EARLY);
  SB();
  asm volatile("" ::"s"(w2), "s"(out), "s"(ws));
  const int t2 = (xr >> 1) * 55 + (xr & 1) * 28 + kk;
  const bool has2 = kk < 28 - (xr & 1);
  const int rt = t2 / 5, nl = t2 - rt * 5, nt = 5 * xc + nl;
  LSTAMP(0);
  __amdgpu_buffer_rsrc_t rws = __builtin_amdgcn_make_buffer_rsrc((void*)ws, 0, WS_END, 0x00020000);

  if (__builtin_expect(wid >= 4, 1)) {
    u64 tag;
    asm volatile("global_load_dwordx2 %0, %1, %2 sc1" : "=v"(tag) : "v"(0), "s"(ws + WS_EPOCH) : "memory");
    __amdgpu_buffer_rsrc_t r2 = __builtin_amdgcn_make_buffer_rsrc((void*)w2, 0, F_ * D_ * 2, 0x00020000);
    const int v2 = has2 ? ((lane >> 3) + 16 * L) * 1280 + nt * 128 + (((lane & 7) ^ ((lane >> 4) << 1)) << 4) : OOB;
    auto wu = [&](int p) {
      const int so = unit(p) * (64 * 1280);
      DMA(r2, w2slot(p) * 8192 + L * 2048, v2, so, A2);
      DMA(r2, w2slot(p) * 8192 + L * 2048 + 1024, v2, so + 8 * 1280, A2);
    };
    const int v1b = ct < 13 ? v1 + 128 : OOB;
    int kc = krot;
    auto g1b = [&](int hv) {
      DMA(r1, w1bslot(hv) * 8192 + L * 2048, v1b, kc * (64 * 3456), A1);
      DMA(r1, w1bslot(hv) * 8192 + L * 2048 + 1024, v1b ^ 64, kc * (64 * 3456) + 8 * 3456, A1);
      kc = (kc == 9) ? 0 : kc + 1;
    };
    g1b(0); g1b(1); g1b(2);
    BAR(); g1(EARLY);
    WAITV(12); BAR(); g1(EARLY);
    WAITV(6); BAR(); g1(EARLY);
#pragma unroll 1
    for (int v = 3; v < 8; ++v) { WAITV(6); BAR(); g1(LATE); }
    WAITV(6); BAR(); g1b(3); g1b(4); g1b(5); g1b(6);
    WAITV(8); BAR(); g1b(7); g1b(8);
    LSTAMP(1);
    WAITV(8); BAR(); g1b(9); wu(7); wu(8); wu(9);
    WAITV(6); BAR();
    wu(10); wu(11); wu(12); wu(13); wu(14); wu(15);
    BAR();
    if (!has2) { WAITV(0); DUMP(); return; }
    wu(16); wu(0); wu(1); wu(2); wu(3); wu(4); wu(5); wu(6);
    WAITV(14); BAR();
    LSTAMP(2);
    asm volatile("" : "+v"(tag));
    tag += 1;
    const int mb0 = 3 * rt;
    const bool fl0 = lane < 48 && (lane & 15) < 14, fl1 = lane < 48 && (lane & 15) < 13;
    const GAS u64* fp = (const GAS u64*)(ws + WS_HFLAG + min(mb0 + min(lane >> 4, 2), 63) * 128 + (lane & 15) * 8);
    u64 fs;
    auto poll = [&](int half) {
      if (!__all(fs == tag || !(half ? fl1 : fl0))) {
        for (unsigned spin = 0; spin < SPIN_MAX; ++spin) {
          const u64 f = __hip_atomic_load(fp + half * 1024, RLX_AGENT);
          if (__all(f == tag || !(half ? fl1 : fl0))) break;
          __builtin_amdgcn_s_sleep(PSLEEP);
        }
      }
      __builtin_amdgcn_fence(__ATOMIC_ACQUIRE, "wavefront");
    };
    asm volatile("global_load_dwordx2 %0, %1, off sc1" : "=v"(fs) : "v"(fp) : "memory");
    wu(17); wu(18); wu(19);
    asm volatile("s_waitcnt vmcnt(6)" : "+v"(fs)::"memory");
    BAR();
    LSTAMP(7);
    poll(0);
    LSTAMP(3);
    const unsigned hs0 = WS_H + (mb0 * 108 + L) * 1024, hs1 = WS_H + (min(mb0 + 1, 63) * 108 + L) * 1024, hs2 = WS_H + (min(mb0 + 2, 63) * 108 + L) * 1024;
    unsigned hbase = 65536u + L * 1024u;
    auto hs = [&](int g) {
#pragma unroll
      for (int e = 0; e < 2; ++e) {
        const unsigned o = (unsigned)unit(min(2 * g + e, 26)) * 4096u;
        const int vv = (2 * g + e < 27) ? lane * 16 : OOB;
        DMA(rws, hbase + e * 12288, vv, hs0 + o, 16);
        DMA(rws, hbase + e * 12288 + 4096, mb0 < 63 ? vv : OOB, hs1 + o, 16);
        DMA(rws, hbase + e * 12288 + 8192, mb0 < 63 ? vv : OOB, hs2 + o, 16);
      }
      hbase = (hbase >= 65536u + 3 * 24576u) ? hbase - 3 * 24576u : hbase + 24576u;
    };
    hs(0); hs(1);
    WAITV(12); BAR();
#pragma unroll 1
    for (int g = 0; g < 4; ++g) { WAITV(6); BAR(); hs(g + 2); }
    WAITV(6); BAR();
    asm volatile("global_load_dwordx2 %0, %1, off sc1" : "=v"(fs) : "v"(fp + 1024) : "memory");
    wu(20); wu(21); hs(6);
    asm volatile("s_waitcnt vmcnt(6)" : "+v"(fs)::"memory");
    BAR();
    LSTAMP(5); poll(1); LSTAMP(6);
    wu(22); wu(23); hs(7);
    WAITV(6); BAR(); wu(24); hs(8);
    WAITV(6); BAR(); wu(25); hs(9);
    WAITV(6); BAR(); wu(26); hs(10);
#pragma unroll 1
    for (int g = 9; g < 12; ++g) { WAITV(6); BAR(); hs(g + 2); }
    WAITV(6); BAR();
    WAITV(0); BAR();
    LSTAMP(4);
    DUMP();
    return;
  }
  const unsigned lds0 = (unsigned)(size_t)(LAS char*)smem;
  const int w = wid, fr = lane & 15, g4 = lane >> 4, q4 = fr >> 2, p4 = lane & 3, swb = (q4 >> 1) | ((g4 & 1) << 1);
#define PRIO 3
  __builtin_amdgcn_s_setprio(PRIO);
  const u64 tag = __hip_atomic_load((GAS u64*)(ws + WS_EPOCH), RLX_AGENT) + 1;
  asm volatile("" ::"v"(tag));
  const int fo = lane == 0 ? (int)(WS_HFLAG + (i * 4 + w) * 128 + ct * 8) : OOB;
#define FLAG(half) __builtin_amdgcn_raw_buffer_store_b64(__builtin_bit_cast(u32x2, tag), rws, fo, (half) * 8192, 16)
  const int fob = (ct < 13) ? fo : OOB;
  const int kh = w >> 1, nb = w & 1;
  bf16x8 wq[26];
  const unsigned w2a = lds0 + (32 * kh + 4 * g4 + q4) * 128 + (((((2 * nb) ^ swb) << 1) | (p4 >> 1)) << 4) + (p4 & 1) * 8;
  unsigned w2w[3][2];
#define W2O(n) (w2slot((n) >> 1) * 8192)
#define RW2(n) tr2(w2w[W2O(n) >> 16][(n) & 1] + (W2O(n) & 65535), w2w[W2O(n) >> 16][(n) & 1] + (W2O(n) & 65535) + 2048)
  const unsigned hdst = WS_H + ((i * 4 + w) * 108 + ct * 8) * 1024;
  const int hvo = (ct < 13) ? lane * 16 : OOB;
  u32x4 hb[4] = {};
#define PUBB(f) __builtin_amdgcn_raw_buffer_store_b128(hb[f], rws, hvo, hdst + (4 + (f)) * 1024, 16)
  {
    f32x4 aa[4][2], ab[4][2];
    unsigned xa[3][2], wa[3][4];
#pragma unroll
    for (int ks = 2; ks < 6; ++ks) {
      xa[ks >> 1][ks & 1] = lds0 + (ks >> 1) * 65536 + (32 * w + fr) * 128 + (((4 * (ks & 1) + g4) ^ (fr >> 1)) << 4);
      asm volatile("" : "+v"(xa[ks >> 1][ks & 1]));
    }
#pragma unroll
    for (int c = 0; c < 12; ++c) {
      wa[c >> 2][c & 3] = lds0 + (c >> 2) * 65536 + (8 * g4 + q4) * 128 + (((((c & 3) ^ swb) << 1) | (p4 >> 1)) << 4) + (p4 & 1) * 8;
      asm volatile("" : "+v"(wa[c >> 2][c & 3]));
    }
    bf16x8 xf[40], W[5];
#define PACK(acc, f) u32x4{relupk(acc[(f) & 2][(f) & 1][0], acc[(f) & 2][(f) & 1][1]), relupk(acc[(f) & 2][(f) & 1][2], acc[(f) & 2][(f) & 1][3]), \
      relupk(acc[((f) & 2) + 1][(f) & 1][0], acc[((f) & 2) + 1][(f) & 1][1]), relupk(acc[((f) & 2) + 1][(f) & 1][2], acc[((f) & 2) + 1][(f) & 1][3])}
#define PUB(acc, f, half) __builtin_amdgcn_raw_buffer_store_b128(PACK(acc, f), rws, lane * 16, hdst + ((half) * 4 + (f)) * 1024, 16)
    WAITV(0); BAR();
    LSTAMP(1); CSTAMP(12);
    s16x4 wlo;
    auto rd = [&](auto jc, auto hc) {
      constexpr int j = decltype(jc)::value, part = decltype(hc)::value; constexpr Op o = op1(j);
      constexpr int off = (o.half ? w1bslot(o.v) * 8192 : 65536 + (o.v & 3) * 24576 + 16384) + (o.ks >> 1) * 4096, fi = (o.ks & 1) * 2 + o.c;
      constexpr int xo = 65536 + (o.v & 3) * 24576;
      if constexpr (part == 0) {
        if constexpr (o.first && o.ks == 0 && o.v > 0) BAR();
        if constexpr (j == NOP1 || j == NOP1 + 16 || j == NOP1 + 40) BAR();
        if constexpr (o.first && !(o.ks & 1)) xf[o.v * 4 + o.ks] = lds128(xa[xo >> 16][o.ks >> 1] + (xo & 65535));
        wlo = tr1(wa[off >> 16][fi] + (off & 65535));
      } else {
        if constexpr (o.first && !(o.ks & 1)) xf[o.v * 4 + o.ks + 1] = lds128(xa[xo >> 16][o.ks >> 1] + (xo & 65535) + 2048);
        const s16x4 whi = tr1(wa[off >> 16][fi] + (off & 65535) + 512);
        W[j % 5] = __builtin_bit_cast(bf16x8, __builtin_shufflevector(wlo, whi, 0, 1, 2, 3, 4, 5, 6, 7));
      }
    };
    constexpr std::integral_constant<int, 0> P0{}; constexpr std::integral_constant<int, 1> P1{};
    sfor<4>([&](auto jc) { SB(); rd(jc, P0); SB(); rd(jc, P1); });
    sfor<160>([&](auto jc) {
      constexpr int j = decltype(jc)::value; constexpr Op o = op1(j);
      constexpr int fi = (o.ks & 1) * 2 + o.c, xi = o.v * 4 + (o.ks & 2);
      constexpr bool z1 = o.v == 0 && o.ks < 2;
      SB();
      if constexpr (o.half) MMA(z1, ab[fi][0], W[j % 5], xf[xi]); else MMA(z1, aa[fi][0], W[j % 5], xf[xi]);
      SB();
      if constexpr (j + 4 < 160) rd(std::integral_constant<int, j + 4>{}, P0);
      SB();
      if constexpr (o.half) MMA(z1, ab[fi][1], W[j % 5], xf[xi + 1]); else MMA(z1, aa[fi][1], W[j % 5], xf[xi + 1]);
      SB();
      if constexpr (j + 4 < 160) rd(std::integral_constant<int, j + 4>{}, P1);
      SB();
      if constexpr (j == 8) __builtin_amdgcn_raw_buffer_store_b64(__builtin_bit_cast(u32x2, tag), rws, (w == 0 && lane == 0) ? (int)(WS_ACK + b * 8) : OOB, 0, 16);
      if constexpr (j > NOP1 && j <= NOP1 + 8 && ((j - NOP1) & 1) == 0) PUB(aa, (j - NOP1) / 2 - 1, 0);
      if constexpr (j == NOP1) { LSTAMP(7); }
      if constexpr (j == FLAGA_AT) { WAITV(0); FLAG(0); LSTAMP(8); }
    });
    SB(); MFMA_DRAIN(); SB();
    LSTAMP(2); CSTAMP(13);
    hb[0] = PACK(ab, 0); hb[1] = PACK(ab, 1); hb[2] = PACK(ab, 2); hb[3] = PACK(ab, 3);
    if (FLAGA_AT >= 160) { WAITV(0); FLAG(0); LSTAMP(8); }
    if (!has2) {
      PUBB(0); PUBB(1); PUBB(2); PUBB(3);
      WAITV(0);
      if (ct < 13) FLAG(1);
      DUMP();
      return;
    }
  }
  PUBB(0); PUBB(1); PUBB(2); PUBB(3);
#pragma unroll
  for (int c = 0; c < 6; ++c) { w2w[c >> 1][c & 1] = (w2a ^ ((c & 1) * 32)) + (c >> 1) * 65536; asm volatile("" : "+v"(w2w[c >> 1][c & 1])); }
  BAR();
#pragma unroll
  for (int n = 0; n < 20; ++n) wq[n] = RW2(14 + n);
  WAITL0(); BAR();
  WAITV(0);
  __builtin_amdgcn_raw_buffer_store_b64(__builtin_bit_cast(u32x2, tag), rws, fob, 8192, 16);
  LSTAMP(3);
  BAR();
#pragma unroll
  for (int n = 20; n < 26; ++n) wq[n] = RW2(14 + n);
  WAITL0();
  f32x4 a2[3][2][2];
  unsigned ha[2][3][2];
#pragma unroll
  for (int c = 0; c < 12; ++c) {
    const int c6 = c % 6;
    ha[c / 6][c6 >> 1][c6 & 1] = lds0 + 65536 + (c / 6) * 49152 + kh * 2048 + lane * 16 + (kh ? 2 - (c6 >> 1) : (c6 >> 1)) * 4096 + ((c6 & 1) ^ kh) * 1024;
    asm volatile("" : "+v"(ha[c / 6][c6 >> 1][c6 & 1]));
  }
  bf16x8 H[3][3], Wr[2][2];
#define RH(m, n) H[(n) % 3][m] = lds128(ha[((n) >> 3) & 1][m][(n) & 1] + (((n) >> 2) & 1) * 24576 + (((n) >> 1) & 1) * 12288)
  BAR();
  LSTAMP(4); CSTAMP(14);
  SB(); Wr[0][0] = RW2(0); SB(); RH(0, 0); SB(); Wr[0][1] = RW2(1); SB(); RH(1, 0); SB(); RH(2, 0); SB(); RH(0, 1); SB(); RH(1, 1); SB(); RH(2, 1); SB();
  sfor<54>([&](auto nc) {
    constexpr int n = decltype(nc)::value, m = n + 2, s = n % 3, e = n & 1, u = n >> 1;
    constexpr bool reg = u >= 7 && u < 20;
    const bf16x8 w0 = reg ? wq[reg ? 2 * u - 14 : 0] : Wr[u & 1][0], w1f = reg ? wq[reg ? 2 * u - 13 : 0] : Wr[u & 1][1];
    MMA(n < 2, a2[0][e][0], w0, H[s][0]); SB();
    if constexpr (m < 54 && (m & 3) == 0) BAR();
    if constexpr (m < 54) RH(0, m);
    SB();
    MMA(n < 2, a2[0][e][1], w1f, H[s][0]); SB();
    if constexpr (e == 0 && (m < 14 || (m >= 40 && m < 54))) Wr[(m >> 1) & 1][0] = RW2(m);
    SB();
    MMA(n < 2, a2[1][e][0], w0, H[s][1]); SB();
    if constexpr (m < 54) RH(1, m);
    SB();
    MMA(n < 2, a2[1][e][1], w1f, H[s][1]); SB();
    if constexpr (e == 0 && (m < 14 || (m >= 40 && m < 54))) Wr[(m >> 1) & 1][1] = RW2(m + 1);
    SB();
    MMA(n < 2, a2[2][e][0], w0, H[s][2]); SB();
    MMA(n < 2, a2[2][e][1], w1f, H[s][2]); SB();
    if constexpr (m < 54) RH(2, m);
    SB();
  });
  MFMA_DRAIN(); SB();
  LSTAMP(5); CSTAMP(15);
  {
    const unsigned xb = lds0 + 65536 + 2 * 24576 + nb * 12288 + lane * 16;
#pragma unroll
    for (int t = 0; t < 6; ++t) *(LAS f32x4*)(xb + kh * 6144 + t * 1024) = a2[(5 - (t >> 1)) >> 1][(5 - (t >> 1)) & 1][t & 1];
    __amdgpu_buffer_rsrc_t ro = __builtin_amdgcn_make_buffer_rsrc((void*)out, 0, M_ * D_ * 2, 0x00020000);
    const int ob = (96 * rt + (kh ? 80 : 0) + fr) * 1280 + (64 * nt + 32 * nb + (g4 & 1) * 16 + (g4 >> 1) * 8) * 2;
    WAITL0(); BAR();
    f32x4 o[6];
#pragma unroll
    for (int t = 0; t < 6; ++t) o[t] = *(LAS f32x4*)(xb + (kh ^ 1) * 6144 + t * 1024);
#pragma unroll
    for (int t = 0; t < 3; ++t) {
      const f32x4 v0 = o[2 * t] + a2[t >> 1][t & 1][0], v1 = o[2 * t + 1] + a2[t >> 1][t & 1][1];
      auto s0 = __builtin_amdgcn_permlane16_swap(cvtpk(v0[0], v0[1]), cvtpk(v1[0], v1[1]), false, false);
      auto s1 = __builtin_amdgcn_permlane16_swap(cvtpk(v0[2], v0[3]), cvtpk(v1[2], v1[3]), false, false);
      u32x4 ov = {s0[0], s1[0], s0[1], s1[1]};
      __builtin_amdgcn_raw_buffer_store_b128(ov, ro, ob + (kh ? -t : t) * (16 * 1280), 0, OAUX);
    }
  }
  LSTAMP(6);
  DUMP();
#endif
}

extern "C" void kernel_launch(void* const* d_in, const int* in_sizes, int n_in, void* d_out, int out_size, void* d_ws,
                              size_t ws_size, hipStream_t stream) {
  (void)hipFuncSetAttribute((const void*)ffn_fused, hipFuncAttributeMaxDynamicSharedMemorySize, LDS_BYTES);
  ffn_fused<<<256, 512, LDS_BYTES, stream>>>((const char*)d_in[0], (const char*)d_in[1], (const char*)d_in[2], (char*)d_out, (char*)d_ws);
}
```

```cpp
#include <hip/hip_runtime.h>
#include <utility>
typedef short s16x4 __attribute__((ext_vector_type(4)));
typedef short s16x8 __attribute__((ext_vector_type(8)));
typedef __bf16 bf16x8 __attribute__((ext_vector_type(8)));
typedef __bf16 bf16x2 __attribute__((ext_vector_type(2)));
typedef float f32x2 __attribute__((ext_vector_type(2)));
typedef float f32x4 __attribute__((ext_vector_type(4)));
typedef unsigned u32x2 __attribute__((ext_vector_type(2)));
typedef unsigned u32x4 __attribute__((ext_vector_type(4)));
typedef unsigned long long u64;
#define LAS __attribute__((address_space(3)))
#define GAS __attribute__((address_space(1)))
#define DI __device__ __forceinline__

constexpr int M_ = 2048, D_ = 640, F_ = 1728;
constexpr unsigned LDS_BYTES = 163840;
constexpr unsigned WS_EPOCH = 0;
constexpr unsigned WS_ACK = 4096;
constexpr unsigned WS_HFLAG = 8192;
constexpr unsigned WS_H = 1u << 20;
constexpr unsigned WS_END = WS_H + 64 * 108 * 1024;
constexpr unsigned SPIN_MAX = 1u << 16;
constexpr int FLAGA_AT = 136;
constexpr int CODE_BYTES = 20480;
constexpr int OOB = 0x7ffffff0;

#define WAITV(n) asm volatile("s_waitcnt vmcnt(" #n ")" ::: "memory")
#define WAITL0() asm volatile("s_waitcnt lgkmcnt(0)" ::: "memory")
#define BAR() __builtin_amdgcn_s_barrier()
#define SB() __builtin_amdgcn_sched_barrier(0)

template <class F, int... Is> DI void sfor_(F&& f, std::integer_sequence<int, Is...>) { (f(std::integral_constant<int, Is>{}), ...); }
template <int N, class F> DI void sfor(F&& f) { sfor_(f, std::make_integer_sequence<int, N>{}); }
DI unsigned cvtpk(float lo, float hi) { f32x2 v = {lo, hi}; bf16x2 r = __builtin_convertvector(v, bf16x2); return __builtin_bit_cast(unsigned, r); }
DI unsigned relupk(float lo, float hi) {
  typedef short s16x2 __attribute__((ext_vector_type(2)));
  const s16x2 v = __builtin_bit_cast(s16x2, cvtpk(lo, hi)), z = {0, 0};
  return __builtin_bit_cast(unsigned, __builtin_elementwise_max(v, z));
}
DI bf16x8 tr2(unsigned a0, unsigned a1) {
  s16x4 lo = __builtin_amdgcn_ds_read_tr16_b64_v4i16((LAS s16x4*)a0);
  s16x4 hi = __builtin_amdgcn_ds_read_tr16_b64_v4i16((LAS s16x4*)a1);
  s16x8 v = __builtin_shufflevector(lo, hi, 0, 1, 2, 3, 4, 5, 6, 7);
  return __builtin_bit_cast(bf16x8, v);
}
DI bf16x8 lds128(unsigned a) { return *(LAS bf16x8*)a; }
#define MMA(first, c, a, b) do { if constexpr (first) asm volatile("v_mfma_f32_16x16x32_bf16 %0, %1, %2, 0" : "=&v"(c) : "v"(a), "v"(b)); \
    else asm volatile("v_mfma_f32_16x16x32_bf16 %0, %1, %2, %0" : "+v"(c) : "v"(a), "v"(b)); } while (0)
#define MFMA_DRAIN() asm volatile("s_nop 7\n\ts_nop 1" ::: "memory")
DI s16x4 tr1(unsigned a) { return __builtin_amdgcn_ds_read_tr16_b64_v4i16((LAS s16x4*)a); }
#define RLX_AGENT __ATOMIC_RELAXED, __HIP_MEMORY_SCOPE_AGENT
#define DMA(rsrc, ldsoff, voff, soff, aux) __builtin_amdgcn_raw_ptr_buffer_load_lds(rsrc, (LAS void*)(smem + (ldsoff)), 16, voff, soff, 0, aux)
__host__ __device__ constexpr int unit(int p) { return p < 14 ? 2 * p : 2 * (p - 14) + 1; }
constexpr int NB = 3, NOP1 = 80 + 8 * NB;
struct Op { int v, ks, half, c, first; };
__host__ __device__ constexpr Op op1(int j) {
  if (j >= NOP1) { const int r = j - NOP1; return Op{NB + (r >> 3), (r >> 1) & 3, 1, r & 1, 0}; }
  if (j < 16) return Op{j >> 3, (j >> 1) & 3, 0, j & 1, !(j & 1)};
  if (j < 16 + 16 * NB) { const int r = j - 16, st = 2 + (r >> 4), h = (r >> 1) & 1; return Op{h ? st - 2 : st, (r >> 2) & 3, h, r & 1, (r & 3) == 0}; }
  const int r = j - 16 - 16 * NB; return Op{2 + NB + (r >> 3), (r >> 1) & 3, 0, r & 1, !(r & 1)};
}
__host__ __device__ constexpr int w2slot(int p) { return p < 7 ? p : p < 11 ? p + 9 : p < 17 ? p - 3 : p == 17 ? 7 : p < 20 ? p - 4 : p - 20; }
__host__ __device__ constexpr int w1bslot(int hv) { return hv < 8 ? hv : 6 + hv; }

DI void helper(int b, int lane, int wid, const char* w2, char* ws, char* smem) {
#if defined(__HIP_DEVICE_COMPILE__)
  const int xc = b & 1, hw = wid * 4 + ((b >> 3) - 28);
  LAS void* sink = (LAS void*)(smem + wid * 256);
  if (hw < 3) {
    u64 pc; unsigned po;
    asm volatile("s_getpc_b64 %0\n.Lpc%=:\n\ts_mov_b32 %1, .Lpc%=-ffn_fused" : "=s"(pc), "=s"(po));
    __amdgpu_buffer_rsrc_t rc = __builtin_amdgcn_make_buffer_rsrc((void*)(pc - po), 0, CODE_BYTES, 0x00020000);
    __builtin_amdgcn_raw_ptr_buffer_load_lds(rc, sink, 4, (hw * 64 + lane) * 128, 0, 0, 0);
  }
  __amdgpu_buffer_rsrc_t r2 = __builtin_amdgcn_make_buffer_rsrc((void*)w2, 0, F_ * D_ * 2, 0x00020000);
#pragma unroll 1
  for (int t = hw; t < 135; t += 32) {
    const int uo = t / 5, c = t - uo * 5, u = unit(uo < 10 ? uo + 7 : uo < 17 ? uo - 10 : uo);
    __builtin_amdgcn_raw_ptr_buffer_load_lds(r2, sink, 4, lane * 1280, u * (64 * 1280) + xc * 640 + c * 128, 0, 0);
  }
  if (wid != 7) { WAITV(0); return; }
  const u64 tag = __hip_atomic_load((GAS u64*)(ws + WS_EPOCH), RLX_AGENT) + 1;
  if (lane == 0) __hip_atomic_store((GAS u64*)(ws + WS_ACK + b * 8), tag, RLX_AGENT);
  if (b == 255) {
    for (unsigned spin = 0; spin < SPIN_MAX; ++spin) {
      bool ok = true;
#pragma unroll
      for (int q = 0; q < 4; ++q) ok &= __hip_atomic_load((GAS u64*)(ws + WS_ACK + (q * 64 + lane) * 8), RLX_AGENT) == tag;
      if (__all(ok)) break;
      __builtin_amdgcn_s_sleep(8);
    }
    if (lane == 0) __hip_atomic_store((GAS u64*)(ws + WS_EPOCH), tag, RLX_AGENT);
  }
  WAITV(0);
#endif
}

extern "C" __global__ __launch_bounds__(512, 2) void ffn_fused(const char* __restrict__ x, const char* __restrict__ w1, const char* __restrict__ w2,
                                                    char* __restrict__ out, char* __restrict__ ws) {
#if defined(__HIP_DEVICE_COMPILE__)
  extern __shared__ __attribute__((aligned(16))) char smem[];
  const int b = blockIdx.x, xr = (b & 7) >> 1, xc = b & 1, kk = b >> 3;
  const int lane = threadIdx.x & 63, wid = __builtin_amdgcn_readfirstlane(threadIdx.x >> 6);
  if (__builtin_expect(kk >= 28, 0)) { helper(b, lane, wid, w2, ws, smem); return; }
  const int i = 4 * xr + (kk & 3), cl = kk >> 2, ct = 7 * xc + cl;
  const int L = wid & 3;
  const int wsw = ((lane & 7) ^ (((lane >> 4) & 1) << 1)) << 4;
  int krot = (((kk & 3) + cl) % 3) * 3 + (xr >> 1) + xc;
  const int vx0 = (128 * i + 32 * L + (lane >> 3)) * 1280 + (((lane & 7) ^ (lane >> 4)) << 4), vx1 = (vx0 + 8 * 1280) ^ 64;
  const int v1 = ((lane >> 3) + 16 * L) * 3456 + ct * 256 + wsw;
  __amdgpu_buffer_rsrc_t rx = __builtin_amdgcn_make_buffer_rsrc((void*)x, 0, M_ * D_ * 2, 0x00020000);
  __amdgpu_buffer_rsrc_t r1 = __builtin_amdgcn_make_buffer_rsrc((void*)w1, 0, D_ * F_ * 2, 0x00020000);
  int kb = krot + (wid >> 2); kb -= kb >= 10 ? 10 : 0;
  unsigned base = 65536u + (wid >> 2) * 24576u;
  auto g1 = [&]() {
    DMA(rx, base + L * 4096, vx0, kb * 128, 0);
    DMA(rx, base + L * 4096 + 1024, vx1, kb * 128, 0);
    DMA(rx, base + L * 4096 + 2048, vx0, kb * 128 + 16 * 1280, 0);
    DMA(rx, base + L * 4096 + 3072, vx1, kb * 128 + 16 * 1280, 0);
    DMA(r1, base + 16384 + L * 2048, v1, kb * (64 * 3456), 0);
    DMA(r1, base + 16384 + L * 2048 + 1024, v1 ^ 64, kb * (64 * 3456) + 8 * 3456, 0);
    kb = (kb == 9) ? 0 : kb + 1;
    base = (base == 65536u + 3 * 24576u) ? 65536u : base + 24576u;
  };
  g1();
  SB();
  asm volatile("" ::"s"(w2), "s"(out), "s"(ws));
  const int t2 = (xr >> 1) * 55 + (xr & 1) * 28 + kk;
  const bool has2 = kk < 28 - (xr & 1);
  const int rt = t2 / 5, nl = t2 - rt * 5, nt = 5 * xc + nl;
  __amdgpu_buffer_rsrc_t rws = __builtin_amdgcn_make_buffer_rsrc((void*)ws, 0, WS_END, 0x00020000);

  if (__builtin_expect(wid >= 4, 1)) {
    u64 tag;
    asm volatile("global_load_dwordx2 %0, %1, %2 sc1" : "=v"(tag) : "v"(0), "s"(ws + WS_EPOCH) : "memory");
    __amdgpu_buffer_rsrc_t r2 = __builtin_amdgcn_make_buffer_rsrc((void*)w2, 0, F_ * D_ * 2, 0x00020000);
    const int v2 = has2 ? ((lane >> 3) + 16 * L) * 1280 + nt * 128 + (((lane & 7) ^ ((lane >> 4) << 1)) << 4) : OOB;
    auto wu = [&](int p) {
      const int so = unit(p) * (64 * 1280);
      DMA(r2, w2slot(p) * 8192 + L * 2048, v2, so, 0);
      DMA(r2, w2slot(p) * 8192 + L * 2048 + 1024, v2, so + 8 * 1280, 0);
    };
    const int v1b = ct < 13 ? v1 + 128 : OOB;
    int kc = krot;
    auto g1b = [&](int hv) {
      DMA(r1, w1bslot(hv) * 8192 + L * 2048, v1b, kc * (64 * 3456), 0);
      DMA(r1, w1bslot(hv) * 8192 + L * 2048 + 1024, v1b ^ 64, kc * (64 * 3456) + 8 * 3456, 0);
      kc = (kc == 9) ? 0 : kc + 1;
    };
    g1b(0); g1b(1); g1b(2);
    BAR(); g1();
    WAITV(12); BAR(); g1();
#pragma unroll 1
    for (int v = 2; v < 8; ++v) { WAITV(6); BAR(); g1(); }
    WAITV(6); BAR(); g1b(3); g1b(4); g1b(5); g1b(6);
    WAITV(8); BAR(); g1b(7); g1b(8);
    WAITV(8); BAR(); g1b(9); wu(7); wu(8); wu(9);
    WAITV(6); BAR();
    wu(10); wu(11); wu(12); wu(13); wu(14); wu(15);
    BAR();
    if (!has2) { WAITV(0); return; }
    wu(16); wu(0); wu(1); wu(2); wu(3); wu(4); wu(5); wu(6);
    WAITV(14); BAR();
    asm volatile("" : "+v"(tag));
    tag += 1;
    const int mb0 = 3 * rt;
    const bool fl0 = lane < 48 && (lane & 15) < 14, fl1 = lane < 48 && (lane & 15) < 13;
    const GAS u64* fp = (const GAS u64*)(ws + WS_HFLAG + min(mb0 + min(lane >> 4, 2), 63) * 128 + (lane & 15) * 8);
    u64 fs;
    auto poll = [&](int half) {
      if (!__all(fs == tag || !(half ? fl1 : fl0))) {
        for (unsigned spin = 0; spin < SPIN_MAX; ++spin) {
          const u64 f = __hip_atomic_load(fp + half * 1024, RLX_AGENT);
          if (__all(f == tag || !(half ? fl1 : fl0))) break;
          __builtin_amdgcn_s_sleep(1);
        }
      }
      __builtin_amdgcn_fence(__ATOMIC_ACQUIRE, "wavefront");
    };
    asm volatile("global_load_dwordx2 %0, %1, off sc1" : "=v"(fs) : "v"(fp) : "memory");
    wu(17); wu(18); wu(19);
    asm volatile("s_waitcnt vmcnt(6)" : "+v"(fs)::"memory");
    BAR();
    poll(0);
    const unsigned hs0 = WS_H + (mb0 * 108 + L) * 1024, hs1 = WS_H + (min(mb0 + 1, 63) * 108 + L) * 1024, hs2 = WS_H + (min(mb0 + 2, 63) * 108 + L) * 1024;
    unsigned hbase = 65536u + L * 1024u;
    auto hs = [&](int g) {
#pragma unroll
      for (int e = 0; e < 2; ++e) {
        const unsigned o = (unsigned)unit(min(2 * g + e, 26)) * 4096u;
        const int vv = (2 * g + e < 27) ? lane * 16 : OOB;
        DMA(rws, hbase + e * 12288, vv, hs0 + o, 16);
        DMA(rws, hbase + e * 12288 + 4096, mb0 < 63 ? vv : OOB, hs1 + o, 16);
        DMA(rws, hbase + e * 12288 + 8192, mb0 < 63 ? vv : OOB, hs2 + o, 16);
      }
      hbase = (hbase >= 65536u + 3 * 24576u) ? hbase - 3 * 24576u : hbase + 24576u;
    };
    hs(0); hs(1);
    WAITV(12); BAR();
#pragma unroll 1
    for (int g = 0; g < 4; ++g) { WAITV(6); BAR(); hs(g + 2); }
    WAITV(6); BAR();
    asm volatile("global_load_dwordx2 %0, %1, off sc1" : "=v"(fs) : "v"(fp + 1024) : "memory");
    wu(20); wu(21); hs(6);
    asm volatile("s_waitcnt vmcnt(6)" : "+v"(fs)::"memory");
    BAR();
    poll(1);
    wu(22); wu(23); hs(7);
    WAITV(6); BAR(); wu(24); hs(8);
    WAITV(6); BAR(); wu(25); hs(9);
    WAITV(6); BAR(); wu(26); hs(10);
#pragma unroll 1
    for (int g = 9; g < 12; ++g) { WAITV(6); BAR(); hs(g + 2); }
    WAITV(6); BAR();
    WAITV(0); BAR();

    return;
  }
  const unsigned lds0 = (unsigned)(size_t)(LAS char*)smem;
  const int w = wid, fr = lane & 15, g4 = lane >> 4, q4 = fr >> 2, p4 = lane & 3, swb = (q4 >> 1) | ((g4 & 1) << 1);
  __builtin_amdgcn_s_setprio(3);
  const u64 tag = __hip_atomic_load((GAS u64*)(ws + WS_EPOCH), RLX_AGENT) + 1;
  asm volatile("" ::"v"(tag));
  const int fo = lane == 0 ? (int)(WS_HFLAG + (i * 4 + w) * 128 + ct * 8) : OOB;
#define FLAG(half) __builtin_amdgcn_raw_buffer_store_b64(__builtin_bit_cast(u32x2, tag), rws, fo, (half) * 8192, 16)
  const int fob = (ct < 13) ? fo : OOB;
  const int kh = w >> 1, nb = w & 1;
  bf16x8 wq[26];
  const unsigned w2a = lds0 + (32 * kh + 4 * g4 + q4) * 128 + (((((2 * nb) ^ swb) << 1) | (p4 >> 1)) << 4) + (p4 & 1) * 8;
  unsigned w2w[3][2];
#define W2O(n) (w2slot((n) >> 1) * 8192)
#define RW2(n) tr2(w2w[W2O(n) >> 16][(n) & 1] + (W2O(n) & 65535), w2w[W2O(n) >> 16][(n) & 1] + (W2O(n) & 65535) + 2048)
  const unsigned hdst = WS_H + ((i * 4 + w) * 108 + ct * 8) * 1024;
  const int hvo = (ct < 13) ? lane * 16 : OOB;
  u32x4 hb[4] = {};
#define PUBB(f) __builtin_amdgcn_raw_buffer_store_b128(hb[f], rws, hvo, hdst + (4 + (f)) * 1024, 16)
  {
    f32x4 aa[4][2], ab[4][2];
    unsigned xa[3][2], wa[3][4];
#pragma unroll
    for (int ks = 2; ks < 6; ++ks) {
      xa[ks >> 1][ks & 1] = lds0 + (ks >> 1) * 65536 + (32 * w + fr) * 128 + (((4 * (ks & 1) + g4) ^ (fr >> 1)) << 4);
      asm volatile("" : "+v"(xa[ks >> 1][ks & 1]));
    }
#pragma unroll
    for (int c = 0; c < 12; ++c) {
      wa[c >> 2][c & 3] = lds0 + (c >> 2) * 65536 + (8 * g4 + q4) * 128 + (((((c & 3) ^ swb) << 1) | (p4 >> 1)) << 4) + (p4 & 1) * 8;
      asm volatile("" : "+v"(wa[c >> 2][c & 3]));
    }
    bf16x8 xf[40], W[5];
#define PACK(acc, f) u32x4{relupk(acc[(f) & 2][(f) & 1][0], acc[(f) & 2][(f) & 1][1]), relupk(acc[(f) & 2][(f) & 1][2], acc[(f) & 2][(f) & 1][3]), \
      relupk(acc[((f) & 2) + 1][(f) & 1][0], acc[((f) & 2) + 1][(f) & 1][1]), relupk(acc[((f) & 2) + 1][(f) & 1][2], acc[((f) & 2) + 1][(f) & 1][3])}
#define PUB(acc, f, half) __builtin_amdgcn_raw_buffer_store_b128(PACK(acc, f), rws, lane * 16, hdst + ((half) * 4 + (f)) * 1024, 16)
    WAITV(0); BAR();
    s16x4 wlo;
    auto rd = [&](auto jc, auto hc) {
      constexpr int j = decltype(jc)::value, part = decltype(hc)::value; constexpr Op o = op1(j);
      constexpr int off = (o.half ? w1bslot(o.v) * 8192 : 65536 + (o.v & 3) * 24576 + 16384) + (o.ks >> 1) * 4096, fi = (o.ks & 1) * 2 + o.c;
      constexpr int xo = 65536 + (o.v & 3) * 24576;
      if constexpr (part == 0) {
        if constexpr (o.first && o.ks == 0 && o.v > 0) BAR();
        if constexpr (j == NOP1 || j == NOP1 + 16 || j == NOP1 + 40) BAR();
        if constexpr (o.first && !(o.ks & 1)) xf[o.v * 4 + o.ks] = lds128(xa[xo >> 16][o.ks >> 1] + (xo & 65535));
        wlo = tr1(wa[off >> 16][fi] + (off & 65535));
      } else {
        if constexpr (o.first && !(o.ks & 1)) xf[o.v * 4 + o.ks + 1] = lds128(xa[xo >> 16][o.ks >> 1] + (xo & 65535) + 2048);
        const s16x4 whi = tr1(wa[off >> 16][fi] + (off & 65535) + 512);
        W[j % 5] = __builtin_bit_cast(bf16x8, __builtin_shufflevector(wlo, whi, 0, 1, 2, 3, 4, 5, 6, 7));
      }
    };
    constexpr std::integral_constant<int, 0> P0{}; constexpr std::integral_constant<int, 1> P1{};
    sfor<4>([&](auto jc) { SB(); rd(jc, P0); SB(); rd(jc, P1); });
    sfor<160>([&](auto jc) {
      constexpr int j = decltype(jc)::value; constexpr Op o = op1(j);
      constexpr int fi = (o.ks & 1) * 2 + o.c, xi = o.v * 4 + (o.ks & 2);
      constexpr bool z1 = o.v == 0 && o.ks < 2;
      SB();
      if constexpr (o.half) MMA(z1, ab[fi][0], W[j % 5], xf[xi]); else MMA(z1, aa[fi][0], W[j % 5], xf[xi]);
      SB();
      if constexpr (j + 4 < 160) rd(std::integral_constant<int, j + 4>{}, P0);
      SB();
      if constexpr (o.half) MMA(z1, ab[fi][1], W[j % 5], xf[xi + 1]); else MMA(z1, aa[fi][1], W[j % 5], xf[xi + 1]);
      SB();
      if constexpr (j + 4 < 160) rd(std::integral_constant<int, j + 4>{}, P1);
      SB();
      if constexpr (j == 8) __builtin_amdgcn_raw_buffer_store_b64(__builtin_bit_cast(u32x2, tag), rws, (w == 0 && lane == 0) ? (int)(WS_ACK + b * 8) : OOB, 0, 16);
      if constexpr (j > NOP1 && j <= NOP1 + 8 && ((j - NOP1) & 1) == 0) PUB(aa, (j - NOP1) / 2 - 1, 0);
      if constexpr (j == FLAGA_AT) { WAITV(0); FLAG(0); }
    });
    SB(); MFMA_DRAIN(); SB();
    hb[0] = PACK(ab, 0); hb[1] = PACK(ab, 1); hb[2] = PACK(ab, 2); hb[3] = PACK(ab, 3);
    if (!has2) {
      PUBB(0); PUBB(1); PUBB(2); PUBB(3);
      WAITV(0);
      if (ct < 13) FLAG(1);
      return;
    }
  }
  PUBB(0); PUBB(1); PUBB(2); PUBB(3);
#pragma unroll
  for (int c = 0; c < 6; ++c) { w2w[c >> 1][c & 1] = (w2a ^ ((c & 1) * 32)) + (c >> 1) * 65536; asm volatile("" : "+v"(w2w[c >> 1][c & 1])); }
  BAR();
#pragma unroll
  for (int n = 0; n < 20; ++n) wq[n] = RW2(14 + n);
  WAITL0(); BAR();
  WAITV(0);
  __builtin_amdgcn_raw_buffer_store_b64(__builtin_bit_cast(u32x2, tag), rws, fob, 8192, 16);
  BAR();
#pragma unroll
  for (int n = 20; n < 26; ++n) wq[n] = RW2(14 + n);
  WAITL0();
  f32x4 a2[3][2][2];
  unsigned ha[2][3][2];
#pragma unroll
  for (int c = 0; c < 12; ++c) {
    const int c6 = c % 6;
    ha[c / 6][c6 >> 1][c6 & 1] = lds0 + 65536 + (c / 6) * 49152 + kh * 2048 + lane * 16 + (kh ? 2 - (c6 >> 1) : (c6 >> 1)) * 4096 + ((c6 & 1) ^ kh) * 1024;
    asm volatile("" : "+v"(ha[c / 6][c6 >> 1][c6 & 1]));
  }
  bf16x8 H[3][3], Wr[2][2];
#define RH(m, n) H[(n) % 3][m] = lds128(ha[((n) >> 3) & 1][m][(n) & 1] + (((n) >> 2) & 1) * 24576 + (((n) >> 1) & 1) * 12288)
  BAR();
  SB(); Wr[0][0] = RW2(0); SB(); RH(0, 0); SB(); Wr[0][1] = RW2(1); SB(); RH(1, 0); SB(); RH(2, 0); SB(); RH(0, 1); SB(); RH(1, 1); SB(); RH(2, 1); SB();
  sfor<54>([&](auto nc) {
    constexpr int n = decltype(nc)::value, m = n + 2, s = n % 3, e = n & 1, u = n >> 1;
    constexpr bool reg = u >= 7 && u < 20;
    const bf16x8 w0 = reg ? wq[reg ? 2 * u - 14 : 0] : Wr[u & 1][0], w1f = reg ? wq[reg ? 2 * u - 13 : 0] : Wr[u & 1][1];
    MMA(n < 2, a2[0][e][0], w0, H[s][0]); SB();
    if constexpr (m < 54 && (m & 3) == 0) BAR();
    if constexpr (m < 54) RH(0, m);
    SB();
    MMA(n < 2, a2[0][e][1], w1f, H[s][0]); SB();
    if constexpr (e == 0 && (m < 14 || (m >= 40 && m < 54))) Wr[(m >> 1) & 1][0] = RW2(m);
    SB();
    MMA(n < 2, a2[1][e][0], w0, H[s][1]); SB();
    if constexpr (m < 54) RH(1, m);
    SB();
    MMA(n < 2, a2[1][e][1], w1f, H[s][1]); SB();
    if constexpr (e == 0 && (m < 14 || (m >= 40 && m < 54))) Wr[(m >> 1) & 1][1] = RW2(m + 1);
    SB();
    MMA(n < 2, a2[2][e][0], w0, H[s][2]); SB();
    MMA(n < 2, a2[2][e][1], w1f, H[s][2]); SB();
    if constexpr (m < 54) RH(2, m);
    SB();
  });
  MFMA_DRAIN(); SB();
  {
    const unsigned xb = lds0 + 65536 + 2 * 24576 + nb * 12288 + lane * 16;
#pragma unroll
    for (int t = 0; t < 6; ++t) *(LAS f32x4*)(xb + kh * 6144 + t * 1024) = a2[(5 - (t >> 1)) >> 1][(5 - (t >> 1)) & 1][t & 1];
    __amdgpu_buffer_rsrc_t ro = __builtin_amdgcn_make_buffer_rsrc((void*)out, 0, M_ * D_ * 2, 0x00020000);
    const int ob = (96 * rt + (kh ? 80 : 0) + fr) * 1280 + (64 * nt + 32 * nb + (g4 & 1) * 16 + (g4 >> 1) * 8) * 2;
    WAITL0(); BAR();
    f32x4 o[6];
#pragma unroll
    for (int t = 0; t < 6; ++t) o[t] = *(LAS f32x4*)(xb + (kh ^ 1) * 6144 + t * 1024);
#pragma unroll
    for (int t = 0; t < 3; ++t) {
      const f32x4 v0 = o[2 * t] + a2[t >> 1][t & 1][0], v1 = o[2 * t + 1] + a2[t >> 1][t & 1][1];
      auto s0 = __builtin_amdgcn_permlane16_swap(cvtpk(v0[0], v0[1]), cvtpk(v1[0], v1[1]), false, false);
      auto s1 = __builtin_amdgcn_permlane16_swap(cvtpk(v0[2], v0[3]), cvtpk(v1[2], v1[3]), false, false);
      u32x4 ov = {s0[0], s1[0], s0[1], s1[1]};
      __builtin_amdgcn_raw_buffer_store_b128(ov, ro, ob + (kh ? -t : t) * (16 * 1280), 0, 16);
    }
  }

#endif
}

extern "C" void kernel_launch(void* const* d_in, const int* in_sizes, int n_in, void* d_out, int out_size, void* d_ws,
                              size_t ws_size, hipStream_t stream) {
  (void)hipFuncSetAttribute((const void*)ffn_fused, hipFuncAttributeMaxDynamicSharedMemorySize, LDS_BYTES);
  ffn_fused<<<256, 512, LDS_BYTES, stream>>>((const char*)d_in[0], (const char*)d_in[1], (const char*)d_in[2], (char*)d_out, (char*)d_ws);
}
```

```cpp
#include <hip/hip_runtime.h>
#include <utility>
typedef short s16x4 __attribute__((ext_vector_type(4)));
typedef short s16x8 __attribute__((ext_vector_type(8)));
typedef __bf16 bf16x8 __attribute__((ext_vector_type(8)));
typedef __bf16 bf16x2 __attribute__((ext_vector_type(2)));
typedef float f32x2 __attribute__((ext_vector_type(2)));
typedef float f32x4 __attribute__((ext_vector_type(4)));
typedef unsigned u32x2 __attribute__((ext_vector_type(2)));
typedef unsigned u32x4 __attribute__((ext_vector_type(4)));
typedef unsigned long long u64;
#define LAS __attribute__((address_space(3)))
#define GAS __attribute__((address_space(1)))
#define DI __device__ __forceinline__

constexpr int M_ = 2048, D_ = 640, F_ = 1728;
constexpr unsigned LDS_BYTES = 163840;
constexpr unsigned WS_EPOCH = 0;
constexpr unsigned WS_ACK = 4096;
constexpr unsigned WS_HFLAG = 8192;
constexpr unsigned WS_H = 1u << 20;
constexpr unsigned WS_END = WS_H + 64 * 108 * 1024;
constexpr unsigned SPIN_MAX = 1u << 16;
constexpr int FLAGA_AT = 136;
constexpr int CODE_BYTES = 20992;
constexpr int OOB = 0x7ffffff0;

#define WAITV(n) asm volatile("s_waitcnt vmcnt(" #n ")" ::: "memory")
#define WAITL0() asm volatile("s_waitcnt lgkmcnt(0)" ::: "memory")
#define BAR() __builtin_amdgcn_s_barrier()
#define SB() __builtin_amdgcn_sched_barrier(0)

template <class F, int... Is> DI void sfor_(F&& f, std::integer_sequence<int, Is...>) { (f(std::integral_constant<int, Is>{}), ...); }
template <int N, class F> DI void sfor(F&& f) { sfor_(f, std::make_integer_sequence<int, N>{}); }
DI unsigned cvtpk(float lo, float hi) { f32x2 v = {lo, hi}; bf16x2 r = __builtin_convertvector(v, bf16x2); return __builtin_bit_cast(unsigned, r); }
DI unsigned relupk(float lo, float hi) {
  typedef short s16x2 __attribute__((ext_vector_type(2)));
  const s16x2 v = __builtin_bit_cast(s16x2, cvtpk(lo, hi)), z = {0, 0};
  return __builtin_bit_cast(unsigned, __builtin_elementwise_max(v, z));
}
DI bf16x8 tr2(unsigned a0, unsigned a1) {
  s16x4 lo = __builtin_amdgcn_ds_read_tr16_b64_v4i16((LAS s16x4*)a0);
  s16x4 hi = __builtin_amdgcn_ds_read_tr16_b64_v4i16((LAS s16x4*)a1);
  s16x8 v = __builtin_shufflevector(lo, hi, 0, 1, 2, 3, 4, 5, 6, 7);
  return __builtin_bit_cast(bf16x8, v);
}
DI bf16x8 lds128(unsigned a) { return *(LAS bf16x8*)a; }
#define MMA(first, c, a, b) do { if constexpr (first) asm volatile("v_mfma_f32_16x16x32_bf16 %0, %1, %2, 0" : "=&v"(c) : "v"(a), "v"(b)); \
    else asm volatile("v_mfma_f32_16x16x32_bf16 %0, %1, %2, %0" : "+v"(c) : "v"(a), "v"(b)); } while (0)
#define MFMA_DRAIN() asm volatile("s_nop 7\n\ts_nop 1" ::: "memory")
#define PIN2(p, q) asm volatile("" : "+v"(p), "+v"(q))
DI s16x4 tr1(unsigned a) { return __builtin_amdgcn_ds_read_tr16_b64_v4i16((LAS s16x4*)a); }
#define RLX_AGENT __ATOMIC_RELAXED, __HIP_MEMORY_SCOPE_AGENT
#define DMA(rsrc, ldsoff, voff, soff, aux) __builtin_amdgcn_raw_ptr_buffer_load_lds(rsrc, (LAS void*)(smem + (ldsoff)), 16, voff, soff, 0, aux)
__host__ __device__ constexpr int unit(int p) { return p < 14 ? 2 * p : 2 * (p - 14) + 1; }
constexpr int NB = 3, NOP1 = 80 + 8 * NB;
struct Op { int v, ks, half, c, first; };
__host__ __device__ constexpr Op op1(int j) {
  if (j >= NOP1) { const int r = j - NOP1; return Op{NB + (r >> 3), (r >> 1) & 3, 1, r & 1, 0}; }
  if (j < 16) return Op{j >> 3, (j >> 1) & 3, 0, j & 1, !(j & 1)};
  if (j < 16 + 16 * NB) { const int r = j - 16, st = 2 + (r >> 4), h = (r >> 1) & 1; return Op{h ? st - 2 : st, (r >> 2) & 3, h, r & 1, (r & 3) == 0}; }
  const int r = j - 16 - 16 * NB; return Op{2 + NB + (r >> 3), (r >> 1) & 3, 0, r & 1, !(r & 1)};
}
__host__ __device__ constexpr int w2slot(int p) { return p < 7 ? p : p < 11 ? p + 9 : p < 17 ? p - 3 : p == 17 ? 7 : p < 20 ? p - 4 : p - 20; }
__host__ __device__ constexpr int w1bslot(int hv) { return hv < 8 ? hv : 6 + hv; }

DI void helper(int b, int lane, int wid, const char* w2, char* ws, char* smem) {
#if defined(__HIP_DEVICE_COMPILE__)
  const int xc = b & 1, hw = wid * 4 + ((b >> 3) - 28);
  LAS void* sink = (LAS void*)(smem + wid * 256);
  if (hw < 3) {
    u64 pc; unsigned po;
    asm volatile("s_getpc_b64 %0\n.Lpc%=:\n\ts_mov_b32 %1, .Lpc%=-ffn_fused" : "=s"(pc), "=s"(po));
    __amdgpu_buffer_rsrc_t rc = __builtin_amdgcn_make_buffer_rsrc((void*)(pc - po), 0, CODE_BYTES, 0x00020000);
    __builtin_amdgcn_raw_ptr_buffer_load_lds(rc, sink, 4, (hw * 64 + lane) * 128, 0, 0, 0);
  }
  __amdgpu_buffer_rsrc_t r2 = __builtin_amdgcn_make_buffer_rsrc((void*)w2, 0, F_ * D_ * 2, 0x00020000);
#pragma unroll 1
  for (int t = hw; t < 135; t += 32) {
    const int uo = t / 5, c = t - uo * 5, u = unit(uo < 10 ? uo + 7 : uo < 17 ? uo - 10 : uo);
    __builtin_amdgcn_raw_ptr_buffer_load_lds(r2, sink, 4, lane * 1280, u * (64 * 1280) + xc * 640 + c * 128, 0, 0);
  }
  if (b == 255 && wid == 7) {
    const u64 tag = __hip_atomic_load((GAS u64*)(ws + WS_EPOCH), RLX_AGENT) + 1;
    for (unsigned spin = 0; spin < SPIN_MAX; ++spin) {
      bool ok = true;
#pragma unroll
      for (int q = 0; q < 4; ++q) ok &= __hip_atomic_load((GAS u64*)(ws + WS_ACK + min(q * 64 + lane, 223) * 8), RLX_AGENT) == tag;
      if (__all(ok)) break;
      __builtin_amdgcn_s_sleep(8);
    }
    if (lane == 0) __hip_atomic_store((GAS u64*)(ws + WS_EPOCH), tag, RLX_AGENT);
  }
  WAITV(0);
#endif
}

extern "C" __global__ __launch_bounds__(512, 2) void ffn_fused(const char* __restrict__ x, const char* __restrict__ w1, const char* __restrict__ w2,
                                                    char* __restrict__ out, char* __restrict__ ws) {
#if defined(__HIP_DEVICE_COMPILE__)
  extern __shared__ __attribute__((aligned(16))) char smem[];
  const int b = blockIdx.x, xr = (b & 7) >> 1, xc = b & 1, kk = b >> 3;
  const int lane = threadIdx.x & 63, wid = __builtin_amdgcn_readfirstlane(threadIdx.x >> 6);
  if (__builtin_expect(kk >= 28, 0)) { helper(b, lane, wid, w2, ws, smem); return; }
  const int i = 4 * xr + (kk & 3), cl = kk >> 2, ct = 7 * xc + cl;
  const int L = wid & 3;
  const int wsw = ((lane & 7) ^ (((lane >> 4) & 1) << 1)) << 4;
  int krot = (((kk & 3) + cl) % 3) * 3 + (xr >> 1) + xc;
  const int vx0 = (128 * i + 32 * L + (lane >> 3)) * 1280 + (((lane & 7) ^ (lane >> 4)) << 4), vx1 = (vx0 + 8 * 1280) ^ 64;
  const int v1 = ((lane >> 3) + 16 * L) * 3456 + ct * 256 + wsw;
  __amdgpu_buffer_rsrc_t rx = __builtin_amdgcn_make_buffer_rsrc((void*)x, 0, M_ * D_ * 2, 0x00020000);
  __amdgpu_buffer_rsrc_t r1 = __builtin_amdgcn_make_buffer_rsrc((void*)w1, 0, D_ * F_ * 2, 0x00020000);
  int kb = krot + (wid >> 2); kb -= kb >= 10 ? 10 : 0;
  unsigned base = 65536u + (wid >> 2) * 24576u;
  auto g1 = [&]() {
    DMA(rx, base + L * 4096, vx0, kb * 128, 0);
    DMA(rx, base + L * 4096 + 1024, vx1, kb * 128, 0);
    DMA(rx, base + L * 4096 + 2048, vx0, kb * 128 + 16 * 1280, 0);
    DMA(rx, base + L * 4096 + 3072, vx1, kb * 128 + 16 * 1280, 0);
    DMA(r1, base + 16384 + L * 2048, v1, kb * (64 * 3456), 0);
    DMA(r1, base + 16384 + L * 2048 + 1024, v1 ^ 64, kb * (64 * 3456) + 8 * 3456, 0);
    kb = (kb == 9) ? 0 : kb + 1;
    base = (base == 65536u + 3 * 24576u) ? 65536u : base + 24576u;
  };
  g1();
  SB();
  asm volatile("" ::"s"(w2), "s"(out), "s"(ws));
  const int t2 = (xr >> 1) * 55 + (xr & 1) * 28 + kk;
  const bool has2 = kk < 28 - (xr & 1);
  const int rt = t2 / 5, nl = t2 - rt * 5, nt = 5 * xc + nl;
  __amdgpu_buffer_rsrc_t rws = __builtin_amdgcn_make_buffer_rsrc((void*)ws, 0, WS_END, 0x00020000);

  if (__builtin_expect(wid >= 4, 1)) {
    u64 tag;
    asm volatile("global_load_dwordx2 %0, %1, %2 sc1" : "=v"(tag) : "v"(0), "s"(ws + WS_EPOCH) : "memory");
    __amdgpu_buffer_rsrc_t r2 = __builtin_amdgcn_make_buffer_rsrc((void*)w2, 0, F_ * D_ * 2, 0x00020000);
    const int v2 = has2 ? ((lane >> 3) + 16 * L) * 1280 + nt * 128 + (((lane & 7) ^ ((lane >> 4) << 1)) << 4) : OOB;
    auto wu = [&](int p) {
      const int so = unit(p) * (64 * 1280);
      DMA(r2, w2slot(p) * 8192 + L * 2048, v2, so, 0);
      DMA(r2, w2slot(p) * 8192 + L * 2048 + 1024, v2, so + 8 * 1280, 0);
    };
    const int v1b = ct < 13 ? v1 + 128 : OOB;
    int kc = krot;
    auto g1b = [&](int hv) {
      DMA(r1, w1bslot(hv) * 8192 + L * 2048, v1b, kc * (64 * 3456), 0);
      DMA(r1, w1bslot(hv) * 8192 + L * 2048 + 1024, v1b ^ 64, kc * (64 * 3456) + 8 * 3456, 0);
      kc = (kc == 9) ? 0 : kc + 1;
    };
    g1b(0); g1b(1); g1b(2);
    BAR(); g1();
    WAITV(12); BAR(); g1();
#pragma unroll 1
    for (int v = 2; v < 8; ++v) { WAITV(6); BAR(); g1(); }
    WAITV(6); BAR(); g1b(3); g1b(4); g1b(5); g1b(6);
    WAITV(8); BAR(); g1b(7); g1b(8);
    WAITV(8); BAR(); g1b(9); wu(7); wu(8); wu(9);
    WAITV(6); BAR();
    wu(10); wu(11); wu(12); wu(13); wu(14); wu(15);
    BAR();
    if (!has2) { WAITV(0); return; }
    wu(16); wu(0); wu(1); wu(2); wu(3); wu(4); wu(5); wu(6);
    WAITV(14); BAR();
    asm volatile("" : "+v"(tag));
    tag += 1;
    const int mb0 = 3 * rt;
    const bool fl0 = lane < 48 && (lane & 15) < 14, fl1 = lane < 48 && (lane & 15) < 13;
    const GAS u64* fp = (const GAS u64*)(ws + WS_HFLAG + min(mb0 + min(lane >> 4, 2), 63) * 128 + (lane & 15) * 8);
    u64 fs;
    auto poll = [&](int half) {
      if (!__all(fs == tag || !(half ? fl1 : fl0))) {
        for (unsigned spin = 0; spin < SPIN_MAX; ++spin) {
          const u64 f = __hip_atomic_load(fp + half * 1024, RLX_AGENT);
          if (__all(f == tag || !(half ? fl1 : fl0))) break;
          __builtin_amdgcn_s_sleep(1);
        }
      }
      __builtin_amdgcn_fence(__ATOMIC_ACQUIRE, "wavefront");
    };
    asm volatile("global_load_dwordx2 %0, %1, off sc1" : "=v"(fs) : "v"(fp) : "memory");
    wu(17); wu(18); wu(19);
    asm volatile("s_waitcnt vmcnt(6)" : "+v"(fs)::"memory");
    BAR();
    poll(0);
    const unsigned hs0 = WS_H + (mb0 * 108 + L) * 1024, hs1 = WS_H + (min(mb0 + 1, 63) * 108 + L) * 1024, hs2 = WS_H + (min(mb0 + 2, 63) * 108 + L) * 1024;
    unsigned hbase = 65536u + L * 1024u;
    auto hs = [&](int g) {
#pragma unroll
      for (int e = 0; e < 2; ++e) {
        const unsigned o = (unsigned)unit(min(2 * g + e, 26)) * 4096u;
        const int vv = (2 * g + e < 27) ? lane * 16 : OOB;
        DMA(rws, hbase + e * 12288, vv, hs0 + o, 16);
        DMA(rws, hbase + e * 12288 + 4096, mb0 < 63 ? vv : OOB, hs1 + o, 16);
        DMA(rws, hbase + e * 12288 + 8192, mb0 < 63 ? vv : OOB, hs2 + o, 16);
      }
      hbase = (hbase >= 65536u + 3 * 24576u) ? hbase - 3 * 24576u : hbase + 24576u;
    };
    hs(0); hs(1);
    WAITV(12); BAR();
    WAITV(6); BAR(); hs(2); hs(3);
    WAITV(12); BAR(); hs(4);
    WAITV(12); BAR(); hs(5);
    WAITV(12); BAR();
    asm volatile("global_load_dwordx2 %0, %1, off sc1" : "=v"(fs) : "v"(fp + 1024) : "memory");
    hs(6);
    WAITV(13); BAR();
    wu(20); wu(21);
    asm volatile("s_waitcnt vmcnt(10)" : "+v"(fs)::"memory");
    poll(1);
    hs(7);
    WAITV(16); BAR(); wu(22); wu(23); hs(8);
    WAITV(16); BAR(); wu(24); hs(9);
    WAITV(14); BAR(); wu(25); hs(10);
    WAITV(14); BAR(); wu(26); hs(11);
    WAITV(14); BAR(); hs(12);
    WAITV(12); BAR(); hs(13);
    WAITV(12); BAR();
    WAITV(6); BAR();
    WAITV(0); BAR();
    return;
  }
  const unsigned lds0 = (unsigned)(size_t)(LAS char*)smem;
  const int w = wid, fr = lane & 15, g4 = lane >> 4, q4 = fr >> 2, p4 = lane & 3, swb = (q4 >> 1) | ((g4 & 1) << 1);
  __builtin_amdgcn_s_setprio(3);
  const u64 tag = __hip_atomic_load((GAS u64*)(ws + WS_EPOCH), RLX_AGENT) + 1;
  asm volatile("" ::"v"(tag));
  const int fo = lane == 0 ? (int)(WS_HFLAG + (i * 4 + w) * 128 + ct * 8) : OOB;
#define FLAG(half) __builtin_amdgcn_raw_buffer_store_b64(__builtin_bit_cast(u32x2, tag), rws, fo, (half) * 8192, 16)
  const int fob = (ct < 13) ? fo : OOB;
  const int kh = w >> 1, nb = w & 1;
  bf16x8 wq[26];
  const unsigned w2a = lds0 + (32 * kh + 4 * g4 + q4) * 128 + (((((2 * nb) ^ swb) << 1) | (p4 >> 1)) << 4) + (p4 & 1) * 8;
  unsigned w2w[3][2];
#define W2O(n) (w2slot((n) >> 1) * 8192)
#define RW2(n) tr2(w2w[W2O(n) >> 16][(n) & 1] + (W2O(n) & 65535), w2w[W2O(n) >> 16][(n) & 1] + (W2O(n) & 65535) + 2048)
  const unsigned hdst = WS_H + ((i * 4 + w) * 108 + ct * 8) * 1024;
  const int hvo = (ct < 13) ? lane * 16 : OOB;
  u32x4 hb[4] = {};
#define PUBB(f) __builtin_amdgcn_raw_buffer_store_b128(hb[f], rws, hvo, hdst + (4 + (f)) * 1024, 16)
  {
    f32x4 aa[4][2], ab[4][2];
    unsigned xa[3][2], wa[3][4];
#pragma unroll
    for (int ks = 2; ks < 6; ++ks) {
      xa[ks >> 1][ks & 1] = lds0 + (ks >> 1) * 65536 + (32 * w + fr) * 128 + (((4 * (ks & 1) + g4) ^ (fr >> 1)) << 4);
      asm volatile("" : "+v"(xa[ks >> 1][ks & 1]));
    }
#pragma unroll
    for (int c = 0; c < 12; ++c) {
      wa[c >> 2][c & 3] = lds0 + (c >> 2) * 65536 + (8 * g4 + q4) * 128 + (((((c & 3) ^ swb) << 1) | (p4 >> 1)) << 4) + (p4 & 1) * 8;
      asm volatile("" : "+v"(wa[c >> 2][c & 3]));
    }
    bf16x8 xf[40], W[5];
#define PACK(acc, f) u32x4{relupk(acc[(f) & 2][(f) & 1][0], acc[(f) & 2][(f) & 1][1]), relupk(acc[(f) & 2][(f) & 1][2], acc[(f) & 2][(f) & 1][3]), \
      relupk(acc[((f) & 2) + 1][(f) & 1][0], acc[((f) & 2) + 1][(f) & 1][1]), relupk(acc[((f) & 2) + 1][(f) & 1][2], acc[((f) & 2) + 1][(f) & 1][3])}
#define PUB(acc, f, half) do { PIN2(acc[(f) & 2][(f) & 1], acc[((f) & 2) + 1][(f) & 1]); \
      __builtin_amdgcn_raw_buffer_store_b128(PACK(acc, f), rws, lane * 16, hdst + ((half) * 4 + (f)) * 1024, 16); } while (0)
    WAITV(0); BAR();
    s16x4 wlo;
    auto rd = [&](auto jc, auto hc) {
      constexpr int j = decltype(jc)::value, part = decltype(hc)::value; constexpr Op o = op1(j);
      constexpr int off = (o.half ? w1bslot(o.v) * 8192 : 65536 + (o.v & 3) * 24576 + 16384) + (o.ks >> 1) * 4096, fi = (o.ks & 1) * 2 + o.c;
      constexpr int xo = 65536 + (o.v & 3) * 24576;
      if constexpr (part == 0) {
        if constexpr (o.first && o.ks == 0 && o.v > 0) BAR();
        if constexpr (j == NOP1 || j == NOP1 + 16 || j == NOP1 + 40) BAR();
        if constexpr (o.first && !(o.ks & 1)) xf[o.v * 4 + o.ks] = lds128(xa[xo >> 16][o.ks >> 1] + (xo & 65535));
        wlo = tr1(wa[off >> 16][fi] + (off & 65535));
      } else {
        if constexpr (o.first && !(o.ks & 1)) xf[o.v * 4 + o.ks + 1] = lds128(xa[xo >> 16][o.ks >> 1] + (xo & 65535) + 2048);
        const s16x4 whi = tr1(wa[off >> 16][fi] + (off & 65535) + 512);
        W[j % 5] = __builtin_bit_cast(bf16x8, __builtin_shufflevector(wlo, whi, 0, 1, 2, 3, 4, 5, 6, 7));
      }
    };
    constexpr std::integral_constant<int, 0> P0{}; constexpr std::integral_constant<int, 1> P1{};
    sfor<4>([&](auto jc) { SB(); rd(jc, P0); SB(); rd(jc, P1); });
    sfor<160>([&](auto jc) {
      constexpr int j = decltype(jc)::value; constexpr Op o = op1(j);
      constexpr int fi = (o.ks & 1) * 2 + o.c, xi = o.v * 4 + (o.ks & 2);
      constexpr bool z1 = o.v == 0 && o.ks < 2;
      SB();
      if constexpr (o.half) MMA(z1, ab[fi][0], W[j % 5], xf[xi]); else MMA(z1, aa[fi][0], W[j % 5], xf[xi]);
      SB();
      if constexpr (j + 4 < 160) rd(std::integral_constant<int, j + 4>{}, P0);
      SB();
      if constexpr (o.half) MMA(z1, ab[fi][1], W[j % 5], xf[xi + 1]); else MMA(z1, aa[fi][1], W[j % 5], xf[xi + 1]);
      SB();
      if constexpr (j + 4 < 160) rd(std::integral_constant<int, j + 4>{}, P1);
      SB();
      if constexpr (j == 8) __builtin_amdgcn_raw_buffer_store_b64(__builtin_bit_cast(u32x2, tag), rws, (w == 0 && lane == 0) ? (int)(WS_ACK + b * 8) : OOB, 0, 16);
      if constexpr (j > NOP1 && j <= NOP1 + 8 && ((j - NOP1) & 1) == 0) PUB(aa, (j - NOP1) / 2 - 1, 0);
      if constexpr (j == FLAGA_AT) { WAITV(0); FLAG(0); }
    });
    SB(); MFMA_DRAIN(); SB();
#pragma unroll
    for (int c = 0; c < 4; ++c) PIN2(ab[c][0], ab[c][1]);
    hb[0] = PACK(ab, 0); hb[1] = PACK(ab, 1); hb[2] = PACK(ab, 2); hb[3] = PACK(ab, 3);
    if (!has2) {
      PUBB(0); PUBB(1); PUBB(2); PUBB(3);
      WAITV(0);
      if (ct < 13) FLAG(1);
      return;
    }
  }
  PUBB(0); PUBB(1); PUBB(2); PUBB(3);
#pragma unroll
  for (int c = 0; c < 6; ++c) { w2w[c >> 1][c & 1] = (w2a ^ ((c & 1) * 32)) + (c >> 1) * 65536; asm volatile("" : "+v"(w2w[c >> 1][c & 1])); }
  BAR();
#pragma unroll
  for (int n = 0; n < 20; ++n) wq[n] = RW2(14 + n);
  WAITL0(); BAR();
  WAITV(0);
  __builtin_amdgcn_raw_buffer_store_b64(__builtin_bit_cast(u32x2, tag), rws, fob, 8192, 16);
  BAR();
#pragma unroll
  for (int n = 20; n < 26; ++n) wq[n] = RW2(14 + n);
  WAITL0();
  f32x4 a2[3][2][2];
  unsigned ha[2][3][2];
#pragma unroll
  for (int c = 0; c < 12; ++c) {
    const int c6 = c % 6;
    ha[c / 6][c6 >> 1][c6 & 1] = lds0 + 65536 + (c / 6) * 49152 + kh * 2048 + lane * 16 + (kh ? 2 - (c6 >> 1) : (c6 >> 1)) * 4096 + ((c6 & 1) ^ kh) * 1024;
    asm volatile("" : "+v"(ha[c / 6][c6 >> 1][c6 & 1]));
  }
  bf16x8 H[3][3], Wr[2][2];
#define RH(m, n) H[(n) % 3][m] = lds128(ha[((n) >> 3) & 1][m][(n) & 1] + (((n) >> 2) & 1) * 24576 + (((n) >> 1) & 1) * 12288)
  BAR();
  SB(); Wr[0][0] = RW2(0); SB(); RH(0, 0); SB(); Wr[0][1] = RW2(1); SB(); RH(1, 0); SB(); RH(2, 0); SB(); RH(0, 1); SB(); RH(1, 1); SB(); RH(2, 1); SB();
  sfor<54>([&](auto nc) {
    constexpr int n = decltype(nc)::value, m = n + 2, s = n % 3, e = n & 1, u = n >> 1;
    constexpr bool reg = u >= 7 && u < 20;
    const bf16x8 w0 = reg ? wq[reg ? 2 * u - 14 : 0] : Wr[u & 1][0], w1f = reg ? wq[reg ? 2 * u - 13 : 0] : Wr[u & 1][1];
    MMA(n < 2, a2[0][e][0], w0, H[s][0]); SB();
    if constexpr (m < 54 && (m & 3) == 0) { WAITL0(); BAR(); }
    if constexpr (m < 54) RH(0, m);
    SB();
    MMA(n < 2, a2[0][e][1], w1f, H[s][0]); SB();
    if constexpr (e == 0 && (m < 14 || (m >= 40 && m < 54))) Wr[(m >> 1) & 1][0] = RW2(m);
    SB();
    MMA(n < 2, a2[1][e][0], w0, H[s][1]); SB();
    if constexpr (m < 54) RH(1, m);
    SB();
    MMA(n < 2, a2[1][e][1], w1f, H[s][1]); SB();
    if constexpr (e == 0 && (m < 14 || (m >= 40 && m < 54))) Wr[(m >> 1) & 1][1] = RW2(m + 1);
    SB();
    MMA(n < 2, a2[2][e][0], w0, H[s][2]); SB();
    MMA(n < 2, a2[2][e][1], w1f, H[s][2]); SB();
    if constexpr (m < 54) RH(2, m);
    SB();
  });
  MFMA_DRAIN(); SB();
#pragma unroll
  for (int c = 0; c < 6; ++c) PIN2(a2[c >> 1][c & 1][0], a2[c >> 1][c & 1][1]);
  {
    const unsigned xb = lds0 + 65536 + 2 * 24576 + nb * 12288 + lane * 16;
#pragma unroll
    for (int t = 0; t < 6; ++t) *(LAS f32x4*)(xb + kh * 6144 + t * 1024) = a2[(5 - (t >> 1)) >> 1][(5 - (t >> 1)) & 1][t & 1];
    __amdgpu_buffer_rsrc_t ro = __builtin_amdgcn_make_buffer_rsrc((void*)out, 0, M_ * D_ * 2, 0x00020000);
    const int ob = (96 * rt + (kh ? 80 : 0) + fr) * 1280 + (64 * nt + 32 * nb + (g4 & 1) * 16 + (g4 >> 1) * 8) * 2;
    WAITL0(); BAR();
    f32x4 o[6];
#pragma unroll
    for (int t = 0; t < 6; ++t) o[t] = *(LAS f32x4*)(xb + (kh ^ 1) * 6144 + t * 1024);
#pragma unroll
    for (int t = 0; t < 3; ++t) {
      const f32x4 v0 = o[2 * t] + a2[t >> 1][t & 1][0], v1 = o[2 * t + 1] + a2[t >> 1][t & 1][1];
      auto s0 = __builtin_amdgcn_permlane16_swap(cvtpk(v0[0], v0[1]), cvtpk(v1[0], v1[1]), false, false);
      auto s1 = __builtin_amdgcn_permlane16_swap(cvtpk(v0[2], v0[3]), cvtpk(v1[2], v1[3]), false, false);
      u32x4 ov = {s0[0], s1[0], s0[1], s1[1]};
      __builtin_amdgcn_raw_buffer_store_b128(ov, ro, ob + (kh ? -t : t) * (16 * 1280), 0, 16);
    }
  }
#endif
}

extern "C" void kernel_launch(void* const* d_in, const int* in_sizes, int n_in, void* d_out, int out_size, void* d_ws,
                              size_t ws_size, hipStream_t stream) {
  (void)hipFuncSetAttribute((const void*)ffn_fused, hipFuncAttributeMaxDynamicSharedMemorySize, LDS_BYTES);
  ffn_fused<<<256, 512, LDS_BYTES, stream>>>((const char*)d_in[0], (const char*)d_in[1], (const char*)d_in[2], (char*)d_out, (char*)d_ws);
}
```
